```python
import torch

def _softmax(x, axis=-1):
    return torch.softmax(x, dim=axis)
def _gelu(x):
    return torch.nn.functional.gelu(x, approximate="tanh")
def _silu(x):
    return torch.nn.functional.silu(x)
def _sigmoid(x):
    return torch.sigmoid(x)
def _layernorm(x, axis=-1, eps=1e-5):
    m = x.mean(dim=axis, keepdim=True)
    v = x.var(dim=axis, keepdim=True, unbiased=False)
    return (x - m) / torch.sqrt(v + eps)
def _scatter_add(tgt, idx, src, axis=0):
    return tgt.clone().index_add_(axis, idx, src)
def _segment_sum(src, seg, n):
    out = torch.zeros((n,) + src.shape[1:], dtype=src.dtype)
    return out.index_add_(0, seg, src)
def _pad(x, pw):
    flat = tuple(v for pair in reversed(pw) for v in pair)
    return torch.nn.functional.pad(x, flat)

kernel_name = 'sequential_preLN__softmax_full_mha__geglu_ffn__prompt'

def setup_inputs():

    _w = (lambda a: (lambda t: t.to(torch.bfloat16) if t.is_floating_point() else t)(torch.as_tensor(a)))
    def _A_setup():
        B, S, D = 1, 2048, 640
        return {
            "x": _w((lambda *s: torch.randn(*s, dtype=torch.bfloat16) * 0.05)(B, S, D)),
            "wqkv": _w((lambda *s: torch.randn(*s, dtype=torch.bfloat16) * 0.05)(D, 3 * D)),
            "wo": _w((lambda *s: torch.randn(*s, dtype=torch.bfloat16) * 0.05)(D, D)),
        }
    _A_in = _A_setup()

    def _B_setup():
        B, S, D, F = 1, 2048, 640, 1728
        return {
            "x": _w((lambda *s: torch.randn(*s, dtype=torch.bfloat16) * 0.05)(B, S, D)),
            "w_gate": _w((lambda *s: torch.randn(*s, dtype=torch.bfloat16) * 0.05)(D, F)),
            "w_up": _w((lambda *s: torch.randn(*s, dtype=torch.bfloat16) * 0.05)(D, F)),
            "w_down": _w((lambda *s: torch.randn(*s, dtype=torch.bfloat16) * 0.05)(F, D)),
        }
    _B_in = _B_setup()

    return {
        "A_x": _A_in["x"],
        "A_wqkv": _A_in["wqkv"],
        "A_wo": _A_in["wo"],
        "B_x": _B_in["x"],
        "B_w_gate": _B_in["w_gate"],
        "B_w_up": _B_in["w_up"],
        "B_w_down": _B_in["w_down"],
    }

def reference(A_x, A_wqkv, A_wo, B_x, B_w_gate, B_w_up, B_w_down):
    def _A_ref(x, wqkv, wo):
        B, S, D = x.shape
        H = 32; Dh = D // H
        qkv = (lambda x, *d: x.permute(*d))((x @ wqkv).reshape(B, S, 3, H, Dh), 2, 0, 3, 1, 4)
        q, k, v = qkv[0], qkv[1], qkv[2]
        scale = Dh ** -0.5
        scores = torch.einsum("bhqd,bhkd->bhqk", q, k) * scale
        p = _softmax(scores, axis=-1)
        o = torch.einsum("bhqk,bhkd->bhqd", p, v)
        return (lambda x, *d: x.permute(*d))(o, 0, 2, 1, 3).reshape(B, S, D) @ wo

    def _B_ref(x, w_gate, w_up, w_down):
        return (_gelu(x @ w_gate) * (x @ w_up)) @ w_down

    x = A_x
    h = x + _A_ref(_layernorm(x, axis=-1), A_wqkv, A_wo)
    h2 = h + _B_ref(_layernorm(h, axis=-1), B_w_gate, B_w_up, B_w_down)
    return h2
```

```cpp
#include <hip/hip_runtime.h>
#include <hip/hip_bf16.h>
#include <stdint.h>

typedef __attribute__((ext_vector_type(8))) short bf16x8;
typedef __attribute__((ext_vector_type(4))) short bf16x4;
typedef __attribute__((ext_vector_type(16))) float f32x16;
typedef __attribute__((ext_vector_type(4))) float f32x4;
typedef __attribute__((ext_vector_type(2))) float f32x2;
typedef __attribute__((ext_vector_type(4))) unsigned u32x4;
typedef __attribute__((ext_vector_type(2))) unsigned u32x2;
typedef __attribute__((address_space(3))) char lchar;
#define LDSP(T, p) ((__attribute__((address_space(3))) T*)(p))

constexpr int SEQ = 2048, DM = 640, FF = 1728;
constexpr float LN_EPS = 1e-5f;
constexpr float QSCALE = 0.22360679774997896f * 1.4426950408889634f;

constexpr int QT_B = 1280;
constexpr int KV_B = 2624;
constexpr size_t WS_Q = 0;
constexpr size_t WS_KV = WS_Q + (size_t)32 * 64 * QT_B;
constexpr size_t WS_O = WS_KV + (size_t)32 * 64 * KV_B;
constexpr size_t WS_H = WS_O + (size_t)128 * 20 * 1024;
constexpr size_t WS_A = WS_H + (size_t)128 * 20 * 1024;
constexpr size_t WS_WO = WS_A + (size_t)128 * 54 * 1024;
constexpr size_t WS_WGU = WS_WO + (size_t)8 * 20 * 5 * 1024;
constexpr size_t WS_WD = WS_WGU + (size_t)16 * 20 * 14 * 1024;
constexpr size_t WS_CSP = WS_WD + (size_t)8 * 54 * 5 * 1024;
constexpr size_t WS_CS = WS_CSP + (size_t)20 * 3584 * 4;
constexpr size_t WS_ST = WS_CS + (size_t)3584 * 4;

#define MFMA32(a, b, c) __builtin_amdgcn_mfma_f32_32x32x16_bf16(a, b, c, 0, 0, 0)
#define MFMA16(a, b, c) __builtin_amdgcn_mfma_f32_16x16x32_bf16(a, b, c, 0, 0, 0)
#define MFMA32K8(a, b, c) __builtin_amdgcn_mfma_f32_32x32x8bf16_1k(a, b, c, 0, 0, 0)
#define SBAR() __builtin_amdgcn_sched_barrier(0)
#define BARRIER() asm volatile("s_waitcnt lgkmcnt(0)\n\ts_barrier" ::: "memory")
#define WAITV_BARRIER(N) asm volatile("s_waitcnt vmcnt(%0) lgkmcnt(0)\n\ts_barrier" ::"i"(N) : "memory")

__device__ __forceinline__ void dma16(const void* sbase, unsigned voff, unsigned lds) {
  asm volatile("s_mov_b32 m0, %2\n\ts_nop 0\n\tglobal_load_lds_dwordx4 %0, %1" ::"v"(voff), "s"(sbase), "s"(lds) : "memory");
}
__device__ __forceinline__ unsigned pk_bf16(float lo, float hi) {
  typedef __attribute__((ext_vector_type(2))) __bf16 bf2;
  bf2 r = __builtin_convertvector((f32x2){lo, hi}, bf2);
  return __builtin_bit_cast(unsigned, r);
}
__device__ __forceinline__ float bf_lo(unsigned w) { return __uint_as_float(w << 16); }
__device__ __forceinline__ float bf_hi(unsigned w) { return __uint_as_float(w & 0xffff0000u); }
__device__ __forceinline__ bf16x4 tr_read(const lchar* p) {
  return __builtin_amdgcn_ds_read_tr16_b64_v4i16(LDSP(bf16x4, p));
}
__device__ __forceinline__ bf16x8 cat8(bf16x4 a, bf16x4 b) { return __builtin_shufflevector(a, b, 0, 1, 2, 3, 4, 5, 6, 7); }
__device__ __forceinline__ float swap32_add(float v) {
  auto r = __builtin_amdgcn_permlane32_swap(__float_as_uint(v), __float_as_uint(v), false, false);
  return __uint_as_float(r[0]) + __uint_as_float(r[1]);
}

template <bool ISV>
__device__ __forceinline__ void k1_compute(lchar* lds, int wave, int lane, int rt, int ct, char* ws) {
  const int wr = wave >> 1, wc = wave & 1, l31 = lane & 31, hi = lane >> 5;
  unsigned xa[4];
  {
    const int row = wr * 64 + l31, g = hi ^ ((row >> 1) & 7);
#pragma unroll
    for (int ks = 0; ks < 4; ++ks) xa[ks] = row * 128 + 16 * ((2 * ks) ^ g);
  }
  unsigned wa[2];
  {
    const int blk = (lane >> 4) & 1, q = (lane & 15) >> 2, p = lane & 3;
#pragma unroll
    for (int nt = 0; nt < 2; ++nt) wa[nt] = 16384 + (8 * hi + q) * 256 + 16 * (4 * ((wc * 2 + (nt ^ wr)) ^ q) + 2 * blk + (p >> 1)) + 8 * (p & 1);
  }
  f32x16 acc[2][2] = {};
  f32x4 cs = {};
  const short one = ((lane & 15) == ((lane >> 4) & 1)) ? (short)0x3f80 : (short)0;
  const bf16x8 sel = {one, one, one, one, one, one, one, one};
  bf16x8 xf[2][2], wf[2][2];
#define K1_READ(buf, slot, ks)                                                                     \
  do {                                                                                             \
    xf[buf][0] = *LDSP(const bf16x8, lds + (slot) + xa[ks]);                                       \
    xf[buf][1] = *LDSP(const bf16x8, lds + (slot) + xa[ks] + 4096);                                \
    wf[buf][0] = cat8(tr_read(lds + (slot) + wa[0] + (ks) * 4096), tr_read(lds + (slot) + wa[0] + (ks) * 4096 + 1024)); \
    wf[buf][1] = cat8(tr_read(lds + (slot) + wa[1] + (ks) * 4096), tr_read(lds + (slot) + wa[1] + (ks) * 4096 + 1024)); \
  } while (0)
#define K1_MMA(buf)                                                                                \
  do {                                                                                             \
    _Pragma("unroll") for (int nt = 0; nt < 2; ++nt) _Pragma("unroll") for (int mt = 0; mt < 2; ++mt) \
        acc[nt][mt] = ISV ? MFMA32(xf[buf][mt], wf[buf][nt], acc[nt][mt]) : MFMA32(wf[buf][nt], xf[buf][mt], acc[nt][mt]); \
    cs = MFMA16(sel, wf[buf][0], cs);                                            \
  } while (0)
  BARRIER();
  K1_READ(0, 0, 0);
  int slot = 0;
  for (int s = 0; s < 10; ++s) {
    const int nslot = (slot == 4 * 32768) ? 0 : slot + 32768;
    K1_READ(1, slot, 1);
    K1_MMA(0);
    K1_READ(0, slot, 2);
    K1_MMA(1);
    K1_READ(1, slot, 3);
    K1_MMA(0);
    BARRIER();
    if (s < 9) K1_READ(0, nslot, 0);
    K1_MMA(1);
    slot = nslot;
  }
  if (lane < 16) {
    *LDSP(float, lds + 1024 + (wc * 64 + wr * 32 + lane) * 4) = cs[0];
    *LDSP(float, lds + 1024 + (wc * 64 + wr * 32 + 16 + lane) * 4) = cs[1];
  }
  BARRIER();
  const int sec = ct / 5, cbase = (ct - sec * 5) * 128 + wc * 64;
  if (!ISV) {
    char* const base = ws + (sec == 0 ? WS_Q : WS_KV);
    const int tstride = sec == 0 ? QT_B : KV_B;
    const float qs = sec == 0 ? QSCALE : 1.0f;
    float mu[2], rs[2];
#pragma unroll
    for (int mt = 0; mt < 2; ++mt) {
      mu[mt] = *LDSP(const float, lds + (wr * 64 + mt * 32 + l31) * 4);
      rs[mt] = *LDSP(const float, lds + 512 + (wr * 64 + mt * 32 + l31) * 4) * qs;
    }
#pragma unroll
    for (int nt = 0; nt < 2; ++nt)
#pragma unroll
      for (int rg = 0; rg < 4; ++rg) {
        const int cl = (nt ^ wr) * 32 + 8 * rg + 4 * hi;
        const f32x4 c4 = *LDSP(const f32x4, lds + 1024 + (wc * 64 + cl) * 4);
        const int U = (cbase + cl) >> 2, head = (U * 205) >> 10, u = U - head * 5;
        const unsigned inner = (u < 4) ? (u >> 1) * 512 + l31 * 16 + (u & 1) * 8 : 1024 + l31 * 8;
        char* const p = base + (unsigned)((head * 64 + rt * 4 + wr * 2) * tstride) + inner;
#pragma unroll
        for (int mt = 0; mt < 2; ++mt) {
          float v[4];
#pragma unroll
          for (int j = 0; j < 4; ++j) v[j] = rs[mt] * (acc[nt][mt][rg * 4 + j] - mu[mt] * c4[j]);
          *(u32x2*)(p + mt * tstride) = (u32x2){pk_bf16(v[0], v[1]), pk_bf16(v[2], v[3])};
        }
      }
  } else {
#pragma unroll
    for (int nt = 0; nt < 2; ++nt) {
      const int cc = cbase + (nt ^ wr) * 32 + l31, head = (cc * 3277) >> 16, d = cc - head * 20;
      const float c1 = *LDSP(const float, lds + 1024 + (wc * 64 + (nt ^ wr) * 32 + l31) * 4);
      char* const p = ws + WS_KV + (unsigned)((head * 64 + rt * 4 + wr * 2) * KV_B + 1280 + hi * 336 + d * 16);
#pragma unroll
      for (int mt = 0; mt < 2; ++mt)
#pragma unroll
        for (int hf = 0; hf < 2; ++hf) {
          unsigned w[4];
#pragma unroll
          for (int g = 0; g < 2; ++g) {
            const int row = wr * 64 + mt * 32 + 16 * hf + 8 * g + 4 * hi;
            const f32x4 m4 = *LDSP(const f32x4, lds + row * 4), r4 = *LDSP(const f32x4, lds + 512 + row * 4);
            float v[4];
#pragma unroll
            for (int j = 0; j < 4; ++j) v[j] = r4[j] * (acc[nt][mt][hf * 8 + g * 4 + j] - m4[j] * c1);
            w[2 * g] = pk_bf16(v[0], v[1]);
            w[2 * g + 1] = pk_bf16(v[2], v[3]);
          }
          *(u32x4*)(p + mt * KV_B + hf * 672) = (u32x4){w[0], w[1], w[2], w[3]};
          if (d == 0) *(u32x4*)(p + mt * KV_B + hf * 672 + 320) = (u32x4){0x3f803f80u, 0x3f803f80u, 0x3f803f80u, 0x3f803f80u};
        }
    }
  }
#undef K1_READ
#undef K1_MMA
}

__global__ __launch_bounds__(768, 3) void k1_qkv(const char* __restrict__ x, const char* __restrict__ w, char* __restrict__ ws) {
  __shared__ __attribute__((aligned(1024))) char smem[163840];
  lchar* const lds = (lchar*)smem;
  const int lane = threadIdx.x & 63, wave = __builtin_amdgcn_readfirstlane(threadIdx.x >> 6);
  const int xcd = blockIdx.x & 7, bi = blockIdx.x >> 3;
  const int rt = (xcd >> 1) * 4 + (bi & 3), ct = (xcd & 1) * 8 + (bi >> 2);
  if (ct >= 15) return;
  if (wave >= 8) {
    const int lw = wave - 8;
    const unsigned lbase = (unsigned)(uintptr_t)lds;
    const unsigned xv = (lane >> 3) * 1280 + 16 * ((lane & 7) ^ ((4 * lw + (lane >> 4)) & 7));
    const unsigned wv = (lane >> 4) * 3840 + 16 * ((lane & 15) ^ ((lane >> 4) << 2));
    const char* const xb = x + (size_t)(rt * 128 + lw * 8) * 1280;
    const char* const wb = w + (size_t)(lw * 4) * 3840 + ct * 256;
    auto issue = [&](int s, int slot) {
#pragma unroll
      for (int i = 0; i < 4; ++i) {
        dma16(xb + i * 32 * 1280 + s * 128, xv, lbase + slot + (lw + 4 * i) * 1024);
        dma16(wb + (size_t)(s * 64 + i * 16) * 3840, wv, lbase + slot + 16384 + (lw + 4 * i) * 1024);
      }
    };
#pragma unroll
    for (int s = 0; s < 5; ++s) issue(s, s * 32768);
    WAITV_BARRIER(32);
#pragma unroll
    for (int s = 1; s < 6; ++s) {
      WAITV_BARRIER(24);
      issue(s + 4, (s - 1) * 32768);
    }
    WAITV_BARRIER(24);
    WAITV_BARRIER(16);
    WAITV_BARRIER(8);
    WAITV_BARRIER(0);
    BARRIER();
    BARRIER();
  } else if (wave >= 4) {
    const int row = (wave - 4) * 32 + (lane & 31), hi = lane >> 5;
    const unsigned ra = row * 128, rx = (4 * hi) ^ ((row >> 1) & 7);
    float s0 = 0.f, s1 = 0.f, q0 = 0.f, q1 = 0.f;
    int slot = 0;
    BARRIER();
    for (int s = 0; s < 10; ++s) {
      u32x4 v[4];
#pragma unroll
      for (int i = 0; i < 4; ++i) v[i] = *LDSP(const u32x4, lds + slot + ra + 16 * (i ^ rx));
      BARRIER();
#pragma unroll
      for (int i = 0; i < 4; ++i)
#pragma unroll
        for (int j = 0; j < 4; ++j) {
          const float a = bf_lo(v[i][j]), b = bf_hi(v[i][j]);
          asm volatile("v_add_f32 %0, %0, %1" : "+v"(s0) : "v"(a));
          asm volatile("v_add_f32 %0, %0, %1" : "+v"(s1) : "v"(b));
          asm volatile("v_fmac_f32 %0, %1, %1" : "+v"(q0) : "v"(a));
          asm volatile("v_fmac_f32 %0, %1, %1" : "+v"(q1) : "v"(b));
        }
      slot = (slot == 4 * 32768) ? 0 : slot + 32768;
    }
    const float sum = swap32_add(s0 + s1), sq = swap32_add(q0 + q1);
    const float mu = sum * (1.0f / DM), var = fmaxf(sq * (1.0f / DM) - mu * mu, 0.f);
    if (hi == 0) {
      *LDSP(float, lds + row * 4) = mu;
      *LDSP(float, lds + 512 + row * 4) = __builtin_amdgcn_rsqf(var + LN_EPS);
    }
    BARRIER();
  } else {
    if (ct >= 10) k1_compute<true>(lds, wave, lane, rt, ct, ws);
    else k1_compute<false>(lds, wave, lane, rt, ct, ws);
  }
}

constexpr int CHUNK_B = 16 * KV_B;
constexpr int K2_SCR = 3 * CHUNK_B;

__device__ __forceinline__ float max16(const f32x16& s) {
  float a = fmaxf(fmaxf(s[0], s[1]), s[2]), b = fmaxf(fmaxf(s[3], s[4]), s[5]);
  a = fmaxf(fmaxf(a, s[6]), s[7]); b = fmaxf(fmaxf(b, s[8]), s[9]);
  a = fmaxf(fmaxf(a, s[10]), s[11]); b = fmaxf(fmaxf(b, s[12]), s[13]);
  a = fmaxf(fmaxf(a, s[14]), s[15]);
  a = fmaxf(a, b);
  auto r = __builtin_amdgcn_permlane32_swap(__float_as_uint(a), __float_as_uint(a), false, false);
  return fmaxf(__uint_as_float(r[0]), __uint_as_float(r[1]));
}
__device__ __forceinline__ bf16x8 pack8(const f32x16& s, int o) {
  u32x4 w = {pk_bf16(s[o], s[o + 1]), pk_bf16(s[o + 2], s[o + 3]), pk_bf16(s[o + 4], s[o + 5]), pk_bf16(s[o + 6], s[o + 7])};
  return __builtin_bit_cast(bf16x8, w);
}

__device__ __forceinline__ f32x16 attn_slow(const char* kv, bf16x8 q1, bf16x4 q2, int lane) {
  const int l31 = lane & 31, hi = lane >> 5;
  f32x16 o = {};
  float m = -INFINITY;
  for (int t = 0; t < 64; ++t) {
    const char* const p = kv + t * KV_B;
    const bf16x8 k1 = *(const bf16x8*)(p + lane * 16);
    const bf16x4 k2 = *(const bf16x4*)(p + 1024 + l31 * 8);
    const int d = l31 > 20 ? 20 : l31;
    const bf16x8 v1 = *(const bf16x8*)(p + 1280 + hi * 336 + d * 16), v2 = *(const bf16x8*)(p + 1280 + 672 + hi * 336 + d * 16);
    f32x16 s = {};
    s = MFMA32K8(k2, q2, s);
    s = MFMA32(k1, q1, s);
    const float mn = fmaxf(m, max16(s));
    const float al = __builtin_amdgcn_exp2f(m - mn);
    m = mn;
#pragma unroll
    for (int r = 0; r < 16; ++r) { s[r] = __builtin_amdgcn_exp2f(s[r] - mn); o[r] *= al; }
    o = MFMA32(v1, pack8(s, 0), o);
    o = MFMA32(v2, pack8(s, 8), o);
  }
  return o;
}

__global__ __launch_bounds__(512, 2) void k2_attn(const char* __restrict__ wo, const char* __restrict__ wg, const char* __restrict__ wu,
                                                  const char* __restrict__ wd, char* __restrict__ ws) {
  __shared__ __attribute__((aligned(1024))) char smem[K2_SCR + 8 * 4096];
  lchar* const lds = (lchar*)smem;
  const unsigned lbase = (unsigned)(uintptr_t)lds;
  const int lane = threadIdx.x & 63, wave = __builtin_amdgcn_readfirstlane(threadIdx.x >> 6);
  const int l31 = lane & 31, hi = lane >> 5;
  const int head = (blockIdx.x & 7) * 4 + (blockIdx.x >> 6), qt = ((blockIdx.x >> 3) & 7) * 8 + wave;
  const char* const kv = ws + WS_KV + (size_t)head * 64 * KV_B;
  const char* const qp = ws + WS_Q + (size_t)(head * 64 + qt) * QT_B;
  bf16x8 q1;
  bf16x4 q2;
  asm volatile("global_load_dwordx4 %0, %2, %3\n\tglobal_load_dwordx2 %1, %4, %3 offset:1024"
               : "=&v"(q1), "=&v"(q2) : "v"(lane * 16), "s"(qp), "v"(l31 * 8) : "memory");
  auto issue_chunk = [&](int c, int slot) {
#pragma unroll
    for (int i = 0; i < 6; ++i) {
      const int p = (i < 5) ? wave + 8 * i : 40;
      dma16(kv + c * CHUNK_B + p * 1024, lane * 16, lbase + slot + p * 1024);
    }
  };
  issue_chunk(0, 0);
  issue_chunk(1, CHUNK_B);
  issue_chunk(2, 2 * CHUNK_B);
  const int unit = blockIdx.x * 8 + wave;
  int mat, uks, ucg;
  {
    int u = unit < 1820 ? unit : 0;
    mat = u / 540;
    u -= mat * 540;
    const int ncg = mat < 2 ? 27 : 10;
    uks = u / ncg;
    ucg = u - uks * ncg;
    const char* const src = mat == 0 ? wg : mat == 1 ? wu : mat == 2 ? wd : wo;
    const int pitch = mat < 2 ? FF * 2 : DM * 2;
#pragma unroll
    for (int i = 0; i < 4; ++i)
      dma16(src + (size_t)(uks * 32 + i * 8) * pitch + ucg * 128, (lane >> 3) * pitch + (lane & 7) * 16, lbase + K2_SCR + wave * 4096 + i * 1024);
  }
  asm volatile("s_waitcnt vmcnt(20)\n\ts_barrier" : "+v"(q1), "+v"(q2)::"memory");
  if (hi) q2 = (bf16x4){0, 0, 0, 0};
  const lchar* ka = lds + lane * 16;
  const lchar* kb = lds + 1024 + l31 * 8;
  const lchar* va = lds + 1280 + hi * 336 + l31 * 16;
  bf16x8 k1[2], v1[2], v2[2];
  bf16x4 k2[2];
#define RD_K(buf, t) do { k1[buf] = *LDSP(const bf16x8, ka + (t) * KV_B); k2[buf] = *LDSP(const bf16x4, kb + (t) * KV_B); } while (0)
#define RD_V(buf, t) do { v1[buf] = *LDSP(const bf16x8, va + (t) * KV_B); v2[buf] = *LDSP(const bf16x8, va + (t) * KV_B + 672); } while (0)
  RD_K(0, 0);
  RD_K(1, 1);
  RD_V(0, 0);
  f32x16 negm, sc[2], o = {};
  {
    f32x16 s = {};
    s = MFMA32K8(k2[0], q2, s);
    s = MFMA32(k1[0], q1, s);
    const float m = -max16(s);
#pragma unroll
    for (int r = 0; r < 16; ++r) { negm[r] = m; sc[0][r] = s[r] + m; }
  }
  for (int c = 0; c < 4; ++c) {
    const int adv = (c == 2) ? -2 * CHUNK_B : CHUNK_B;
#pragma unroll
    for (int j = 0; j < 16; ++j) {
      const int cur = j & 1, nxt = cur ^ 1;
      if (j == 4 && c == 0) WAITV_BARRIER(16);
      if (j == 14) {
        if (c == 0) WAITV_BARRIER(10);
        else if (c == 1) {
          WAITV_BARRIER(0);
          issue_chunk(3, 0);
          if (unit < 1820) {
            const lchar* const sp = lds + K2_SCR + wave * 4096 + (8 * (lane >> 4) + ((lane & 15) >> 2)) * 128 + (lane & 3) * 8;
            const bf16x8 ones = {0x3f80, 0x3f80, 0x3f80, 0x3f80, 0x3f80, 0x3f80, 0x3f80, 0x3f80};
#pragma unroll
            for (int t = 0; t < 4; ++t) {
              const bf16x8 fr = cat8(tr_read(sp + t * 32), tr_read(sp + t * 32 + 512));
              const int col = ucg * 64 + t * 16 + (lane & 15);
              if (mat < 2) {
                const int wcol = col / 56, f = col - wcol * 56;
                const int tile = (wcol & 1) * 7 + (f < 48 ? (f >> 4) + 3 * mat : 6), r = f < 48 ? (f & 15) : f - 48 + 8 * mat;
                *(bf16x8*)(ws + WS_WGU + (unsigned)((((wcol >> 1) * 20 + uks) * 14 + tile) * 1024 + (lane >> 4) * 256 + r * 16)) = fr;
                const f32x4 cs = MFMA16(ones, fr, ((f32x4){0.f, 0.f, 0.f, 0.f}));
                if (lane < 16) *(float*)(ws + WS_CSP + (unsigned)((uks * 3584 + ((wcol >> 1) * 14 + tile) * 16 + r) * 4)) = cs[0];
              } else {
                const int t16 = ucg * 4 + t, cb = (t16 * 205) >> 10, tile = t16 - cb * 5;
                char* const dst = ws + (mat == 2 ? WS_WD : WS_WO);
                *(bf16x8*)(dst + (unsigned)(((cb * (mat == 2 ? 54 : 20) + uks) * 5 + tile) * 1024 + lane * 16)) = fr;
              }
            }
          }
        } else if (c == 2) WAITV_BARRIER(0);
        ka += adv;
        kb += adv;
      }
      if (j == 15) va += adv;
      sc[nxt] = MFMA32K8(k2[nxt], q2, negm);
      sc[nxt] = MFMA32(k1[nxt], q1, sc[nxt]);
      RD_K(cur, (j + 2) & 15);
      RD_V(nxt, (j + 1) & 15);
#pragma unroll
      for (int r = 0; r < 16; ++r) sc[cur][r] = __builtin_amdgcn_exp2f(sc[cur][r]);
      o = MFMA32(v1[cur], pack8(sc[cur], 0), o);
      o = MFMA32(v2[cur], pack8(sc[cur], 8), o);
    }
  }
#undef RD_K
#undef RD_V
  float l;
  {
    auto r = __builtin_amdgcn_permlane32_swap(__float_as_uint(o[8]), __float_as_uint(o[8]), false, false);
    l = __uint_as_float(r[1]);
    float chk = 0.f;
#pragma unroll
    for (int i = 0; i < 8; ++i) chk += fabsf(o[i]);
    if (!hi) chk += fabsf(o[8]) + fabsf(o[9]) + fabsf(o[10]) + fabsf(o[11]);
    const bool bad = !(l < 0x1p100f) || !(chk < INFINITY);
    if (__builtin_expect(__any(bad), 0)) {
      o = attn_slow(kv, q1, q2, lane);
      auto r2 = __builtin_amdgcn_permlane32_swap(__float_as_uint(o[8]), __float_as_uint(o[8]), false, false);
      l = __uint_as_float(r2[1]);
    }
  }
  const float rl = 1.0f / l;
  const int tok = qt * 32 + l31;
  char* const op = ws + WS_O + (unsigned)((tok >> 4) * 20480 + (tok & 15) * 16);
#pragma unroll
  for (int g = 0; g < 3; ++g) {
    const int U = head * 5 + 2 * g + hi;
    if (g < 2 || !hi)
      *(u32x2*)(op + (U >> 3) * 1024 + ((U >> 1) & 3) * 256 + (U & 1) * 8) =
          (u32x2){pk_bf16(o[4 * g] * rl, o[4 * g + 1] * rl), pk_bf16(o[4 * g + 2] * rl, o[4 * g + 3] * rl)};
  }
}

__global__ __launch_bounds__(512, 2) void k3_proj(const char* __restrict__ x, char* __restrict__ ws) {
  __shared__ __attribute__((aligned(1024))) char smem[100 * 1024];
  lchar* const lds = (lchar*)smem;
  const int lane = threadIdx.x & 63, wave = __builtin_amdgcn_readfirstlane(threadIdx.x >> 6);
  const int rt = (blockIdx.x & 7) * 4 + ((blockIdx.x >> 3) & 3), cb = blockIdx.x >> 5;
  if (wave >= 4) {
    const int lw = wave - 4;
    const unsigned lbase = (unsigned)(uintptr_t)lds;
    const char* const src = ws + WS_WO + (size_t)cb * 100 * 1024;
#pragma unroll
    for (int i = 0; i < 25; ++i) dma16(src + (lw + 4 * i) * 1024, lane * 16, lbase + (lw + 4 * i) * 1024);
    WAITV_BARRIER(18);
    WAITV_BARRIER(12);
    WAITV_BARRIER(6);
    WAITV_BARRIER(0);
    if (lw == 0 && blockIdx.x < 224) {
      const float* const p = (const float*)(ws + WS_CSP) + blockIdx.x * 16 + (lane & 15);
      float s = 0.f;
#pragma unroll
      for (int i = 0; i < 5; ++i) s += p[((lane >> 4) + 4 * i) * 3584];
      s += __shfl_xor(s, 16);
      s += __shfl_xor(s, 32);
      if (lane < 16) ((float*)(ws + WS_CS))[blockIdx.x * 16 + lane] = s;
    }
  } else {
    const int t16 = rt * 4 + wave, tok = t16 * 16 + (lane & 15), G = lane >> 4;
    const char* const ap = ws + WS_O + (size_t)t16 * 20480 + lane * 16;
    bf16x8 a[20];
#pragma unroll
    for (int ks = 0; ks < 20; ++ks) a[ks] = *(const bf16x8*)(ap + ks * 1024);
    u32x2 xr[5];
#pragma unroll
    for (int n = 0; n < 5; ++n) xr[n] = *(const u32x2*)(x + (size_t)tok * 1280 + (cb * 80 + n * 16 + G * 4) * 2);
    f32x4 acc[5] = {};
#pragma unroll
    for (int ks = 0; ks < 20; ++ks) {
      if (ks % 5 == 0) BARRIER();
#pragma unroll
      for (int n = 0; n < 5; ++n) acc[n] = MFMA16(*LDSP(const bf16x8, lds + (ks * 5 + n) * 1024 + lane * 16), a[ks], acc[n]);
    }
    float s = 0.f, q = 0.f;
    char* const hp = ws + WS_H + (size_t)t16 * 20480 + (lane & 15) * 16;
#pragma unroll
    for (int n = 0; n < 5; ++n) {
      const unsigned w0 = pk_bf16(acc[n][0] + bf_lo(xr[n][0]), acc[n][1] + bf_hi(xr[n][0]));
      const unsigned w1 = pk_bf16(acc[n][2] + bf_lo(xr[n][1]), acc[n][3] + bf_hi(xr[n][1]));
      const int U = cb * 20 + n * 4 + G;
      *(u32x2*)(hp + (U >> 3) * 1024 + ((U >> 1) & 3) * 256 + (U & 1) * 8) = (u32x2){w0, w1};
      const float h0 = bf_lo(w0), h1 = bf_hi(w0), h2 = bf_lo(w1), h3 = bf_hi(w1);
      s += (h0 + h1) + (h2 + h3);
      q += (h0 * h0 + h1 * h1) + (h2 * h2 + h3 * h3);
    }
    s += __shfl_xor(s, 16); q += __shfl_xor(q, 16);
    s += __shfl_xor(s, 32); q += __shfl_xor(q, 32);
    if (lane < 16) *(f32x2*)(ws + WS_ST + (size_t)(tok * 8 + cb) * 8) = (f32x2){s, q};
  }
}

constexpr int K4_STAGE = 44 * 1024;
__global__ __launch_bounds__(512, 2) void k4_gu(char* __restrict__ ws) {
  __shared__ __attribute__((aligned(1024))) char smem[3 * K4_STAGE];
  lchar* const lds = (lchar*)smem;
  const int lane = threadIdx.x & 63, wave = __builtin_amdgcn_readfirstlane(threadIdx.x >> 6);
  const int xcd = blockIdx.x & 7, bi = blockIdx.x >> 3;
  const int rt = (xcd >> 2) * 8 + (bi & 7), cb = (xcd & 3) * 4 + (bi >> 3);
  if (wave >= 4) {
    const int lw = wave - 4;
    const unsigned lbase = (unsigned)(uintptr_t)lds;
    const char* const ha = ws + WS_H + (size_t)(rt * 8 + lw * 2) * 20480;
    const char* const wb = ws + WS_WGU + (size_t)cb * 20 * 14 * 1024 + lw * 7 * 1024;
    auto issue = [&](int s, int slot) {
#pragma unroll
      for (int i = 0; i < 4; ++i) dma16(ha + (i >> 1) * 20480 + (2 * s + (i & 1)) * 1024, lane * 16, lbase + slot + (lw * 4 + i) * 1024);
#pragma unroll
      for (int i = 0; i < 7; ++i) dma16(wb + s * 28 * 1024 + i * 1024, lane * 16, lbase + slot + (16 + lw * 7 + i) * 1024);
    };
    issue(0, 0);
    issue(1, K4_STAGE);
    issue(2, 2 * K4_STAGE);
    WAITV_BARRIER(22);
    int slot = 0;
    for (int s = 1; s < 8; ++s) {
      WAITV_BARRIER(11);
      issue(s + 2, slot);
      slot = (slot == 2 * K4_STAGE) ? 0 : slot + K4_STAGE;
    }
    WAITV_BARRIER(11);
    WAITV_BARRIER(0);
  } else {
    const int wr = wave >> 1, wc = wave & 1, G = lane >> 4;
    float mu[4], rs[4];
#pragma unroll
    for (int m = 0; m < 4; ++m) {
      const f32x4 p = *(const f32x4*)(ws + WS_ST + (size_t)(rt * 128 + wr * 64 + m * 16 + (lane & 15)) * 64 + G * 16);
      float s = p[0] + p[2], q = p[1] + p[3];
      s += __shfl_xor(s, 16); q += __shfl_xor(q, 16);
      s += __shfl_xor(s, 32); q += __shfl_xor(q, 32);
      mu[m] = s * (1.0f / DM);
      rs[m] = __builtin_amdgcn_rsqf(fmaxf(q * (1.0f / DM) - mu[m] * mu[m], 0.f) + LN_EPS);
    }
    f32x4 cs[7];
#pragma unroll
    for (int n = 0; n < 7; ++n) cs[n] = *(const f32x4*)(ws + WS_CS + (size_t)((cb * 14 + wc * 7 + n) * 16 + G * 4) * 4);
    f32x4 acc[7][4] = {};
    bf16x8 af[2][4], bf[2][7];
    const lchar* const ab = lds + wr * 8 * 1024 + lane * 16;
    const lchar* const bb = lds + (16 + wc * 7) * 1024 + lane * 16;
#define K4_READ(buf, slot, kk)                                                                             \
  do {                                                                                                     \
    _Pragma("unroll") for (int m = 0; m < 4; ++m) af[buf][m] = *LDSP(const bf16x8, ab + (slot) + (2 * m + (kk)) * 1024); \
    _Pragma("unroll") for (int n = 0; n < 7; ++n) bf[buf][n] = *LDSP(const bf16x8, bb + (slot) + ((kk) * 14 + n) * 1024); \
  } while (0)
#define K4_MMA(buf)                                                                                        \
  _Pragma("unroll") for (int n = 0; n < 7; ++n) _Pragma("unroll") for (int m = 0; m < 4; ++m) acc[n][m] = MFMA16(bf[buf][n], af[buf][m], acc[n][m])
    BARRIER();
    K4_READ(0, 0, 0);
    int slot = 0;
    for (int s = 0; s < 10; ++s) {
      const int nslot = (slot == 2 * K4_STAGE) ? 0 : slot + K4_STAGE;
      K4_READ(1, slot, 1);
      K4_MMA(0);
      if (s < 9) {
        BARRIER();
        K4_READ(0, nslot, 0);
      }
      K4_MMA(1);
      slot = nslot;
    }
#undef K4_READ
#undef K4_MMA
    const int f0 = (cb * 2 + wc) * 56;
    constexpr float C0 = -2.0f * 0.7978845608028654f * 1.4426950408889634f, C1 = C0 * 0.044715f;
    auto geglu = [&](const f32x4& g, const f32x4& u, const f32x4& cg, const f32x4& cu, float m_, float r_) -> u32x2 {
      const float k0 = C0 * r_, k1 = C1 * r_ * r_ * r_, ir = __builtin_amdgcn_rcpf(r_ * r_);
      float o[4];
#pragma unroll
      for (int j = 0; j < 4; ++j) {
        const float gg = g[j] - m_ * cg[j], uu = u[j] - m_ * cu[j];
        const float e = __builtin_amdgcn_exp2f(gg * (k0 + k1 * (gg * gg)));
        o[j] = gg * uu * __builtin_amdgcn_rcpf(ir + e * ir);
      }
      return (u32x2){pk_bf16(o[0], o[1]), pk_bf16(o[2], o[3])};
    };
    char* const ap = ws + WS_A + (size_t)(rt * 8 + wr * 4) * 54 * 1024 + (lane & 15) * 16;
#pragma unroll
    for (int n = 0; n < 3; ++n) {
      const int U = (f0 >> 2) + n * 4 + G;
      const unsigned off = (U >> 3) * 1024 + ((U >> 1) & 3) * 256 + (U & 1) * 8;
#pragma unroll
      for (int m = 0; m < 4; ++m) {
        const u32x2 r = geglu(acc[n][m], acc[n + 3][m], cs[n], cs[n + 3], mu[m], rs[m]);
        if (U < FF / 4) *(u32x2*)(ap + m * 54 * 1024 + off) = r;
      }
    }
    {
      const int hi = lane >> 5, U = (f0 >> 2) + 12 + (G & 1);
      const unsigned off = (U >> 3) * 1024 + ((U >> 1) & 3) * 256 + (U & 1) * 8;
      f32x4 cg, cu;
#pragma unroll
      for (int j = 0; j < 4; ++j) {
        auto r = __builtin_amdgcn_permlane32_swap(__float_as_uint(cs[6][j]), __float_as_uint(cs[6][j]), false, false);
        cg[j] = __uint_as_float(r[0]);
        cu[j] = __uint_as_float(r[1]);
      }
#pragma unroll
      for (int mp = 0; mp < 2; ++mp) {
        f32x4 g, u;
#pragma unroll
        for (int j = 0; j < 4; ++j) {
          auto r = __builtin_amdgcn_permlane32_swap(__float_as_uint(acc[6][2 * mp][j]), __float_as_uint(acc[6][2 * mp + 1][j]), false, false);
          g[j] = __uint_as_float(r[0]);
          u[j] = __uint_as_float(r[1]);
        }
        const u32x2 r = geglu(g, u, cg, cu, hi ? mu[2 * mp + 1] : mu[2 * mp], hi ? rs[2 * mp + 1] : rs[2 * mp]);
        if (U < FF / 4) *(u32x2*)(ap + (2 * mp + hi) * 54 * 1024 + off) = r;
      }
    }
  }
}

constexpr int K5_STAGE = 30 * 1024;
__global__ __launch_bounds__(448, 2) void k5_down(char* __restrict__ ws, char* __restrict__ out) {
  __shared__ __attribute__((aligned(1024))) char smem[5 * K5_STAGE];
  lchar* const lds = (lchar*)smem;
  const int lane = threadIdx.x & 63, wave = __builtin_amdgcn_readfirstlane(threadIdx.x >> 6);
  const int xcd = blockIdx.x & 7, bi = blockIdx.x >> 3;
  const int rt = (xcd >> 1) * 8 + (bi & 7), cb = (xcd & 1) * 4 + (bi >> 3);
  if (wave >= 4) {
    const int lw = wave - 4;
    const unsigned lbase = (unsigned)(uintptr_t)lds;
    const char* const src = ws + WS_WD + (size_t)cb * 270 * 1024;
    auto issue = [&](int s, int slot) {
#pragma unroll
      for (int i = 0; i < 10; ++i) dma16(src + (s * 30 + lw + 3 * i) * 1024, lane * 16, lbase + slot + (lw + 3 * i) * 1024);
    };
#pragma unroll
    for (int s = 0; s < 5; ++s) issue(s, s * K5_STAGE);
    WAITV_BARRIER(40);
#pragma unroll
    for (int s = 1; s < 5; ++s) {
      WAITV_BARRIER(30);
      issue(s + 4, (s - 1) * K5_STAGE);
    }
    WAITV_BARRIER(30);
    WAITV_BARRIER(20);
    WAITV_BARRIER(10);
    WAITV_BARRIER(0);
  } else {
    const int t16 = rt * 4 + wave, tok = t16 * 16 + (lane & 15), G = lane >> 4;
    const char* const ap = ws + WS_A + (size_t)t16 * 54 * 1024 + lane * 16;
    constexpr int PF = 30;
    bf16x8 a[PF];
#pragma unroll
    for (int ks = 0; ks < PF; ++ks) a[ks] = *(const bf16x8*)(ap + ks * 1024);
    u32x2 hr[5];
#pragma unroll
    for (int n = 0; n < 5; ++n) {
      const int U = cb * 20 + n * 4 + G;
      hr[n] = *(const u32x2*)(ws + WS_H + (size_t)t16 * 20480 + (lane & 15) * 16 + (U >> 3) * 1024 + ((U >> 1) & 3) * 256 + (U & 1) * 8);
    }
    f32x4 acc[5] = {};
#pragma unroll
    for (int ks = 0; ks < 54; ++ks) {
      if (ks % 6 == 0) BARRIER();
      const bf16x8 av = a[ks % PF];
      if (ks + PF < 54) a[ks % PF] = *(const bf16x8*)(ap + (ks + PF) * 1024);
#pragma unroll
      for (int n = 0; n < 5; ++n)
        acc[n] = MFMA16(*LDSP(const bf16x8, lds + ((ks / 6) % 5) * K5_STAGE + ((ks % 6) * 5 + n) * 1024 + lane * 16), av, acc[n]);
    }
#pragma unroll
    for (int n = 0; n < 5; ++n) {
      const unsigned w0 = pk_bf16(acc[n][0] + bf_lo(hr[n][0]), acc[n][1] + bf_hi(hr[n][0]));
      const unsigned w1 = pk_bf16(acc[n][2] + bf_lo(hr[n][1]), acc[n][3] + bf_hi(hr[n][1]));
      *(u32x2*)(out + (size_t)tok * 1280 + (cb * 80 + n * 16 + G * 4) * 2) = (u32x2){w0, w1};
    }
  }
}

extern "C" void kernel_launch(void* const* d_in, const int* in_sizes, int n_in, void* d_out, int out_size, void* d_ws, size_t ws_size,
                              hipStream_t stream) {
  const char* x = (const char*)d_in[0];
  const char* wqkv = (const char*)d_in[1];
  const char* wo = (const char*)d_in[2];
  const char* wg = (const char*)d_in[4];
  const char* wu = (const char*)d_in[5];
  const char* wd = (const char*)d_in[6];
  char* ws = (char*)d_ws;
  k1_qkv<<<256, 768, 0, stream>>>(x, wqkv, ws);
  k2_attn<<<256, 512, 0, stream>>>(wo, wg, wu, wd, ws);
  k3_proj<<<256, 512, 0, stream>>>(x, ws);
  k4_gu<<<256, 512, 0, stream>>>(ws);
  k5_down<<<256, 448, 0, stream>>>(ws, (char*)d_out);
}
```

```cpp
#include <hip/hip_runtime.h>
#include <hip/hip_bf16.h>
#include <stdint.h>

typedef __attribute__((ext_vector_type(8))) short bf16x8;
typedef __attribute__((ext_vector_type(4))) short bf16x4;
typedef __attribute__((ext_vector_type(16))) float f32x16;
typedef __attribute__((ext_vector_type(4))) float f32x4;
typedef __attribute__((ext_vector_type(2))) float f32x2;
typedef __attribute__((ext_vector_type(4))) unsigned u32x4;
typedef __attribute__((ext_vector_type(2))) unsigned u32x2;
typedef __attribute__((address_space(3))) char lchar;
#define LDSP(T, p) ((__attribute__((address_space(3))) T*)(p))

constexpr int SEQ = 2048, DM = 640, FF = 1728;
constexpr float LN_EPS = 1e-5f;
constexpr float QSCALE = 0.22360679774997896f * 1.4426950408889634f;

constexpr int QT_B = 1280;
constexpr int KV_B = 2624;
constexpr size_t WS_Q = 0;
constexpr size_t WS_KV = WS_Q + (size_t)32 * 64 * QT_B;
constexpr size_t WS_O = WS_KV + (size_t)32 * 64 * KV_B;
constexpr size_t WS_H = WS_O + (size_t)128 * 20 * 1024;
constexpr size_t WS_A = WS_H + (size_t)128 * 20 * 1024;
constexpr size_t WS_WO = WS_A + (size_t)128 * 54 * 1024;
constexpr size_t WS_WGU = WS_WO + (size_t)8 * 20 * 5 * 1024;
constexpr size_t WS_WD = WS_WGU + (size_t)16 * 20 * 14 * 1024;
constexpr size_t WS_CSP = WS_WD + (size_t)8 * 54 * 5 * 1024;
constexpr size_t WS_CS = WS_CSP + (size_t)20 * 3584 * 4;
constexpr size_t WS_ST = WS_CS + (size_t)3584 * 4;

#define MFMA32(a, b, c) __builtin_amdgcn_mfma_f32_32x32x16_bf16(a, b, c, 0, 0, 0)
#define MFMA16(a, b, c) __builtin_amdgcn_mfma_f32_16x16x32_bf16(a, b, c, 0, 0, 0)
#define MFMA32K8(a, b, c) __builtin_amdgcn_mfma_f32_32x32x8bf16_1k(a, b, c, 0, 0, 0)
#define SBAR() __builtin_amdgcn_sched_barrier(0)
#define BARRIER() asm volatile("s_waitcnt lgkmcnt(0)\n\ts_barrier" ::: "memory")
#define WAITV_BARRIER(N) asm volatile("s_waitcnt vmcnt(%0) lgkmcnt(0)\n\ts_barrier" ::"i"(N) : "memory")

#define STAMP(ws, kid, i)
#define STAMP_DECL
#define STAMP_OUT(ws, kid)

__device__ __forceinline__ void dma16(const void* sbase, unsigned voff, unsigned lds) {
  asm volatile("s_mov_b32 m0, %2\n\ts_nop 0\n\tglobal_load_lds_dwordx4 %0, %1" ::"v"(voff), "s"(sbase), "s"(lds) : "memory");
}
__device__ __forceinline__ unsigned pk_bf16(float lo, float hi) {
  typedef __attribute__((ext_vector_type(2))) __bf16 bf2;
  bf2 r = __builtin_convertvector((f32x2){lo, hi}, bf2);
  return __builtin_bit_cast(unsigned, r);
}
__device__ __forceinline__ float bf_lo(unsigned w) { return __uint_as_float(w << 16); }
__device__ __forceinline__ float bf_hi(unsigned w) { return __uint_as_float(w & 0xffff0000u); }
__device__ __forceinline__ bf16x4 tr_read(const lchar* p) {
  return __builtin_amdgcn_ds_read_tr16_b64_v4i16(LDSP(bf16x4, p));
}
__device__ __forceinline__ bf16x8 cat8(bf16x4 a, bf16x4 b) { return __builtin_shufflevector(a, b, 0, 1, 2, 3, 4, 5, 6, 7); }
__device__ __forceinline__ float swap32_add(float v) {
  auto r = __builtin_amdgcn_permlane32_swap(__float_as_uint(v), __float_as_uint(v), false, false);
  return __uint_as_float(r[0]) + __uint_as_float(r[1]);
}

template <bool ISV>
__device__ __forceinline__ void k1_compute(lchar* lds, int wave, int lane, int rt, int ct, char* ws) {
  STAMP_DECL;
  STAMP(ws, 0, 0);
  const int wr = wave >> 1, wc = wave & 1, l31 = lane & 31, hi = lane >> 5;
  unsigned xa[4];
  {
    const int row = wr * 64 + l31, g = hi ^ ((row >> 1) & 7);
#pragma unroll
    for (int ks = 0; ks < 4; ++ks) xa[ks] = row * 128 + 16 * ((2 * ks) ^ g);
  }
  unsigned wa[2];
  {
    const int blk = (lane >> 4) & 1, q = (lane & 15) >> 2, p = lane & 3;
#pragma unroll
    for (int nt = 0; nt < 2; ++nt) wa[nt] = 16384 + (8 * hi + q) * 256 + 16 * (4 * ((wc * 2 + (nt ^ wr)) ^ q) + 2 * blk + (p >> 1)) + 8 * (p & 1);
  }
  f32x16 acc[2][2] = {};
  f32x4 cs = {};
  const short one = ((lane & 15) == ((lane >> 4) & 1)) ? (short)0x3f80 : (short)0;
  const bf16x8 sel = {one, one, one, one, one, one, one, one};
  bf16x8 xf[2][2], wf[2][2];
#define K1_READ(buf, slot, ks)                                                                     \
  do {                                                                                             \
    xf[buf][0] = *LDSP(const bf16x8, lds + (slot) + xa[ks]);                                       \
    xf[buf][1] = *LDSP(const bf16x8, lds + (slot) + xa[ks] + 4096);                                \
    wf[buf][0] = cat8(tr_read(lds + (slot) + wa[0] + (ks) * 4096), tr_read(lds + (slot) + wa[0] + (ks) * 4096 + 1024)); \
    wf[buf][1] = cat8(tr_read(lds + (slot) + wa[1] + (ks) * 4096), tr_read(lds + (slot) + wa[1] + (ks) * 4096 + 1024)); \
  } while (0)
#define K1_MMA(buf)                                                                                \
  do {                                                                                             \
    _Pragma("unroll") for (int nt = 0; nt < 2; ++nt) _Pragma("unroll") for (int mt = 0; mt < 2; ++mt) \
        acc[nt][mt] = ISV ? MFMA32(xf[buf][mt], wf[buf][nt], acc[nt][mt]) : MFMA32(wf[buf][nt], xf[buf][mt], acc[nt][mt]); \
    cs = MFMA16(sel, wf[buf][0], cs);                                            \
  } while (0)
  BARRIER();
  STAMP(ws, 0, 1);
  K1_READ(0, 0, 0);
  int slot = 0;
  for (int s = 0; s < 10; ++s) {
    const int nslot = (slot == 4 * 32768) ? 0 : slot + 32768;
    K1_READ(1, slot, 1);
    K1_MMA(0);
    K1_READ(0, slot, 2);
    K1_MMA(1);
    K1_READ(1, slot, 3);
    K1_MMA(0);
    BARRIER();
    if (s < 9) K1_READ(0, nslot, 0);
    K1_MMA(1);
    slot = nslot;
  }
  STAMP(ws, 0, 2);
  if (lane < 16) {
    *LDSP(float, lds + 1024 + (wc * 64 + wr * 32 + lane) * 4) = cs[0];
    *LDSP(float, lds + 1024 + (wc * 64 + wr * 32 + 16 + lane) * 4) = cs[1];
  }
  BARRIER();
  STAMP(ws, 0, 3);
  const int sec = ct / 5, cbase = (ct - sec * 5) * 128 + wc * 64;
  if (!ISV) {
    char* const base = ws + (sec == 0 ? WS_Q : WS_KV);
    const int tstride = sec == 0 ? QT_B : KV_B;
    const float qs = sec == 0 ? QSCALE : 1.0f;
    float mu[2], rs[2];
#pragma unroll
    for (int mt = 0; mt < 2; ++mt) {
      mu[mt] = *LDSP(const float, lds + (wr * 64 + mt * 32 + l31) * 4);
      rs[mt] = *LDSP(const float, lds + 512 + (wr * 64 + mt * 32 + l31) * 4) * qs;
    }
#pragma unroll
    for (int nt = 0; nt < 2; ++nt)
#pragma unroll
      for (int rg = 0; rg < 4; ++rg) {
        const int cl = (nt ^ wr) * 32 + 8 * rg + 4 * hi;
        const f32x4 c4 = *LDSP(const f32x4, lds + 1024 + (wc * 64 + cl) * 4);
        const int U = (cbase + cl) >> 2, head = (U * 205) >> 10, u = U - head * 5;
        const unsigned inner = (u < 4) ? (u >> 1) * 512 + l31 * 16 + (u & 1) * 8 : 1024 + l31 * 8;
        char* const p = base + (unsigned)((head * 64 + rt * 4 + wr * 2) * tstride) + inner;
#pragma unroll
        for (int mt = 0; mt < 2; ++mt) {
          float v[4];
#pragma unroll
          for (int j = 0; j < 4; ++j) v[j] = rs[mt] * (acc[nt][mt][rg * 4 + j] - mu[mt] * c4[j]);
          *(u32x2*)(p + mt * tstride) = (u32x2){pk_bf16(v[0], v[1]), pk_bf16(v[2], v[3])};
        }
      }
  } else {
#pragma unroll
    for (int nt = 0; nt < 2; ++nt) {
      const int cc = cbase + (nt ^ wr) * 32 + l31, head = (cc * 3277) >> 16, d = cc - head * 20;
      const float c1 = *LDSP(const float, lds + 1024 + (wc * 64 + (nt ^ wr) * 32 + l31) * 4);
      char* const p = ws + WS_KV + (unsigned)((head * 64 + rt * 4 + wr * 2) * KV_B + 1280 + hi * 336 + d * 16);
#pragma unroll
      for (int mt = 0; mt < 2; ++mt)
#pragma unroll
        for (int hf = 0; hf < 2; ++hf) {
          unsigned w[4];
#pragma unroll
          for (int g = 0; g < 2; ++g) {
            const int row = wr * 64 + mt * 32 + 16 * hf + 8 * g + 4 * hi;
            const f32x4 m4 = *LDSP(const f32x4, lds + row * 4), r4 = *LDSP(const f32x4, lds + 512 + row * 4);
            float v[4];
#pragma unroll
            for (int j = 0; j < 4; ++j) v[j] = r4[j] * (acc[nt][mt][hf * 8 + g * 4 + j] - m4[j] * c1);
            w[2 * g] = pk_bf16(v[0], v[1]);
            w[2 * g + 1] = pk_bf16(v[2], v[3]);
          }
          *(u32x4*)(p + mt * KV_B + hf * 672) = (u32x4){w[0], w[1], w[2], w[3]};
          if (d == 0) *(u32x4*)(p + mt * KV_B + hf * 672 + 320) = (u32x4){0x3f803f80u, 0x3f803f80u, 0x3f803f80u, 0x3f803f80u};
        }
    }
  }
  STAMP(ws, 0, 4);
  STAMP(ws, 0, 5);
  STAMP_OUT(ws, 0);
#undef K1_READ
#undef K1_MMA
}

__global__ __launch_bounds__(768, 3) void k1_qkv(const char* __restrict__ x, const char* __restrict__ w, char* __restrict__ ws) {
  __shared__ __attribute__((aligned(1024))) char smem[163840];
  lchar* const lds = (lchar*)smem;
  const int lane = threadIdx.x & 63, wave = __builtin_amdgcn_readfirstlane(threadIdx.x >> 6);
  const int xcd = blockIdx.x & 7, bi = blockIdx.x >> 3;
  const int rt = (xcd >> 1) * 4 + (bi & 3), ct = (xcd & 1) * 8 + (bi >> 2);
  if (ct >= 15) return;
  if (wave >= 8) {
    const int lw = wave - 8;
    const unsigned lbase = (unsigned)(uintptr_t)lds;
    const unsigned xv = (lane >> 3) * 1280 + 16 * ((lane & 7) ^ ((4 * lw + (lane >> 4)) & 7));
    const unsigned wv = (lane >> 4) * 3840 + 16 * ((lane & 15) ^ ((lane >> 4) << 2));
    const char* const xb = x + (size_t)(rt * 128 + lw * 8) * 1280;
    const char* const wb = w + (size_t)(lw * 4) * 3840 + ct * 256;
    auto issue = [&](int s, int slot) {
#pragma unroll
      for (int i = 0; i < 4; ++i) {
        dma16(xb + i * 32 * 1280 + s * 128, xv, lbase + slot + (lw + 4 * i) * 1024);
        dma16(wb + (size_t)(s * 64 + i * 16) * 3840, wv, lbase + slot + 16384 + (lw + 4 * i) * 1024);
      }
    };
#pragma unroll
    for (int s = 0; s < 5; ++s) issue(s, s * 32768);
    WAITV_BARRIER(32);
#pragma unroll
    for (int s = 1; s < 6; ++s) {
      WAITV_BARRIER(24);
      issue(s + 4, (s - 1) * 32768);
    }
    WAITV_BARRIER(24);
    WAITV_BARRIER(16);
    WAITV_BARRIER(8);
    WAITV_BARRIER(0);
    BARRIER();
    BARRIER();
  } else if (wave >= 4) {
    const int row = (wave - 4) * 32 + (lane & 31), hi = lane >> 5;
    const unsigned ra = row * 128, rx = (4 * hi) ^ ((row >> 1) & 7);
    float s0 = 0.f, s1 = 0.f, q0 = 0.f, q1 = 0.f;
    int slot = 0;
    BARRIER();
    for (int s = 0; s < 10; ++s) {
      u32x4 v[4];
#pragma unroll
      for (int i = 0; i < 4; ++i) v[i] = *LDSP(const u32x4, lds + slot + ra + 16 * (i ^ rx));
      BARRIER();
#pragma unroll
      for (int i = 0; i < 4; ++i)
#pragma unroll
        for (int j = 0; j < 4; ++j) {
          const float a = bf_lo(v[i][j]), b = bf_hi(v[i][j]);
          asm volatile("v_add_f32 %0, %0, %1" : "+v"(s0) : "v"(a));
          asm volatile("v_add_f32 %0, %0, %1" : "+v"(s1) : "v"(b));
          asm volatile("v_fmac_f32 %0, %1, %1" : "+v"(q0) : "v"(a));
          asm volatile("v_fmac_f32 %0, %1, %1" : "+v"(q1) : "v"(b));
        }
      slot = (slot == 4 * 32768) ? 0 : slot + 32768;
    }
    const float sum = swap32_add(s0 + s1), sq = swap32_add(q0 + q1);
    const float mu = sum * (1.0f / DM), var = fmaxf(sq * (1.0f / DM) - mu * mu, 0.f);
    if (hi == 0) {
      *LDSP(float, lds + row * 4) = mu;
      *LDSP(float, lds + 512 + row * 4) = __builtin_amdgcn_rsqf(var + LN_EPS);
    }
    BARRIER();
  } else {
    if (ct >= 10) k1_compute<true>(lds, wave, lane, rt, ct, ws);
    else k1_compute<false>(lds, wave, lane, rt, ct, ws);
  }
}

constexpr int CHUNK_B = 16 * KV_B;
constexpr int K2_SCR = 3 * CHUNK_B;

__device__ __forceinline__ float max16(const f32x16& s) {
  float a = fmaxf(fmaxf(s[0], s[1]), s[2]), b = fmaxf(fmaxf(s[3], s[4]), s[5]);
  a = fmaxf(fmaxf(a, s[6]), s[7]); b = fmaxf(fmaxf(b, s[8]), s[9]);
  a = fmaxf(fmaxf(a, s[10]), s[11]); b = fmaxf(fmaxf(b, s[12]), s[13]);
  a = fmaxf(fmaxf(a, s[14]), s[15]);
  a = fmaxf(a, b);
  auto r = __builtin_amdgcn_permlane32_swap(__float_as_uint(a), __float_as_uint(a), false, false);
  return fmaxf(__uint_as_float(r[0]), __uint_as_float(r[1]));
}
__device__ __forceinline__ bf16x8 pack8(const f32x16& s, int o) {
  u32x4 w = {pk_bf16(s[o], s[o + 1]), pk_bf16(s[o + 2], s[o + 3]), pk_bf16(s[o + 4], s[o + 5]), pk_bf16(s[o + 6], s[o + 7])};
  return __builtin_bit_cast(bf16x8, w);
}

__device__ __forceinline__ f32x16 attn_slow(const char* kv, bf16x8 q1, bf16x4 q2, int lane) {
  const int l31 = lane & 31, hi = lane >> 5;
  f32x16 o = {};
  float m = -INFINITY;
  for (int t = 0; t < 64; ++t) {
    const char* const p = kv + t * KV_B;
    const bf16x8 k1 = *(const bf16x8*)(p + lane * 16);
    const bf16x4 k2 = *(const bf16x4*)(p + 1024 + l31 * 8);
    const int d = l31 > 20 ? 20 : l31;
    const bf16x8 v1 = *(const bf16x8*)(p + 1280 + hi * 336 + d * 16), v2 = *(const bf16x8*)(p + 1280 + 672 + hi * 336 + d * 16);
    f32x16 s = {};
    s = MFMA32K8(k2, q2, s);
    s = MFMA32(k1, q1, s);
    const float mn = fmaxf(m, max16(s));
    const float al = __builtin_amdgcn_exp2f(m - mn);
    m = mn;
#pragma unroll
    for (int r = 0; r < 16; ++r) { s[r] = __builtin_amdgcn_exp2f(s[r] - mn); o[r] *= al; }
    o = MFMA32(v1, pack8(s, 0), o);
    o = MFMA32(v2, pack8(s, 8), o);
  }
  return o;
}

__global__ __launch_bounds__(512, 2) void k2_attn(const char* __restrict__ wo, const char* __restrict__ wg, const char* __restrict__ wu,
                                                  const char* __restrict__ wd, char* __restrict__ ws) {
  __shared__ __attribute__((aligned(1024))) char smem[K2_SCR + 8 * 4096];
  lchar* const lds = (lchar*)smem;
  const unsigned lbase = (unsigned)(uintptr_t)lds;
  const int lane = threadIdx.x & 63, wave = __builtin_amdgcn_readfirstlane(threadIdx.x >> 6);
  STAMP_DECL;
  STAMP(ws, 1, 0);
  const int l31 = lane & 31, hi = lane >> 5;
  const int head = (blockIdx.x & 7) * 4 + (blockIdx.x >> 6), qt = ((blockIdx.x >> 3) & 7) * 8 + wave;
  const char* const kv = ws + WS_KV + (size_t)head * 64 * KV_B;
  const char* const qp = ws + WS_Q + (size_t)(head * 64 + qt) * QT_B;
  bf16x8 q1;
  bf16x4 q2;
  asm volatile("global_load_dwordx4 %0, %2, %3\n\tglobal_load_dwordx2 %1, %4, %3 offset:1024"
               : "=&v"(q1), "=&v"(q2) : "v"(lane * 16), "s"(qp), "v"(l31 * 8) : "memory");
  auto issue_chunk = [&](int c, int slot) {
#pragma unroll
    for (int i = 0; i < 6; ++i) {
      const int p = (i < 5) ? wave + 8 * i : 40;
      dma16(kv + c * CHUNK_B + p * 1024, lane * 16, lbase + slot + p * 1024);
    }
  };
  issue_chunk(0, 0);
  issue_chunk(1, CHUNK_B);
  issue_chunk(2, 2 * CHUNK_B);
  const int unit = blockIdx.x * 8 + wave;
  int mat, uks, ucg;
  {
    int u = unit < 1820 ? unit : 0;
    mat = u / 540;
    u -= mat * 540;
    const int ncg = mat < 2 ? 27 : 10;
    uks = u / ncg;
    ucg = u - uks * ncg;
    const char* const src = mat == 0 ? wg : mat == 1 ? wu : mat == 2 ? wd : wo;
    const int pitch = mat < 2 ? FF * 2 : DM * 2;
#pragma unroll
    for (int i = 0; i < 4; ++i)
      dma16(src + (size_t)(uks * 32 + i * 8) * pitch + ucg * 128, (lane >> 3) * pitch + (lane & 7) * 16, lbase + K2_SCR + wave * 4096 + i * 1024);
  }
  asm volatile("s_waitcnt vmcnt(20)\n\ts_barrier" : "+v"(q1), "+v"(q2)::"memory");
  STAMP(ws, 1, 1);
  if (hi) q2 = (bf16x4){0, 0, 0, 0};
  const lchar* ka = lds + lane * 16;
  const lchar* kb = lds + 1024 + l31 * 8;
  const lchar* va = lds + 1280 + hi * 336 + l31 * 16;
  bf16x8 k1[2], v1[2], v2[2];
  bf16x4 k2[2];
#define RD_K(buf, t) do { k1[buf] = *LDSP(const bf16x8, ka + (t) * KV_B); k2[buf] = *LDSP(const bf16x4, kb + (t) * KV_B); } while (0)
#define RD_V(buf, t) do { v1[buf] = *LDSP(const bf16x8, va + (t) * KV_B); v2[buf] = *LDSP(const bf16x8, va + (t) * KV_B + 672); } while (0)
  RD_K(0, 0);
  RD_K(1, 1);
  RD_V(0, 0);
  f32x16 negm, sc[2], o = {};
  {
    f32x16 s = {};
    s = MFMA32K8(k2[0], q2, s);
    s = MFMA32(k1[0], q1, s);
    const float m = -max16(s);
#pragma unroll
    for (int r = 0; r < 16; ++r) { negm[r] = m; sc[0][r] = s[r] + m; }
  }
  for (int c = 0; c < 4; ++c) {
    const int adv = (c == 2) ? -2 * CHUNK_B : CHUNK_B;
#pragma unroll
    for (int j = 0; j < 16; ++j) {
      const int cur = j & 1, nxt = cur ^ 1;
      if (j == 4 && c == 0) WAITV_BARRIER(16);
      if (j == 14) {
        if (c == 0) WAITV_BARRIER(10);
        else if (c == 1) {
          WAITV_BARRIER(0);
          issue_chunk(3, 0);
          if (unit < 1820) {
            const lchar* const sp = lds + K2_SCR + wave * 4096 + (8 * (lane >> 4) + ((lane & 15) >> 2)) * 128 + (lane & 3) * 8;
            const bf16x8 ones = {0x3f80, 0x3f80, 0x3f80, 0x3f80, 0x3f80, 0x3f80, 0x3f80, 0x3f80};
#pragma unroll
            for (int t = 0; t < 4; ++t) {
              const bf16x8 fr = cat8(tr_read(sp + t * 32), tr_read(sp + t * 32 + 512));
              const int col = ucg * 64 + t * 16 + (lane & 15);
              if (mat < 2) {
                const int wcol = col / 56, f = col - wcol * 56;
                const int tile = (wcol & 1) * 7 + (f < 48 ? (f >> 4) + 3 * mat : 6), r = f < 48 ? (f & 15) : f - 48 + 8 * mat;
                *(bf16x8*)(ws + WS_WGU + (unsigned)((((wcol >> 1) * 20 + uks) * 14 + tile) * 1024 + (lane >> 4) * 256 + r * 16)) = fr;
                const f32x4 cs = MFMA16(ones, fr, ((f32x4){0.f, 0.f, 0.f, 0.f}));
                if (lane < 16) *(float*)(ws + WS_CSP + (unsigned)((uks * 3584 + ((wcol >> 1) * 14 + tile) * 16 + r) * 4)) = cs[0];
              } else {
                const int t16 = ucg * 4 + t, cb = (t16 * 205) >> 10, tile = t16 - cb * 5;
                char* const dst = ws + (mat == 2 ? WS_WD : WS_WO);
                *(bf16x8*)(dst + (unsigned)(((cb * (mat == 2 ? 54 : 20) + uks) * 5 + tile) * 1024 + lane * 16)) = fr;
              }
            }
          }
        } else if (c == 2) WAITV_BARRIER(0);
        ka += adv;
        kb += adv;
      }
      if (j == 15) va += adv;
      sc[nxt] = MFMA32K8(k2[nxt], q2, negm);
      sc[nxt] = MFMA32(k1[nxt], q1, sc[nxt]);
      RD_K(cur, (j + 2) & 15);
      RD_V(nxt, (j + 1) & 15);
#pragma unroll
      for (int r = 0; r < 16; ++r) sc[cur][r] = __builtin_amdgcn_exp2f(sc[cur][r]);
      o = MFMA32(v1[cur], pack8(sc[cur], 0), o);
      o = MFMA32(v2[cur], pack8(sc[cur], 8), o);
    }
  }
  STAMP(ws, 1, 2);
#undef RD_K
#undef RD_V
  float l;
  {
    auto r = __builtin_amdgcn_permlane32_swap(__float_as_uint(o[8]), __float_as_uint(o[8]), false, false);
    l = __uint_as_float(r[1]);
    float chk = 0.f;
#pragma unroll
    for (int i = 0; i < 8; ++i) chk += fabsf(o[i]);
    if (!hi) chk += fabsf(o[8]) + fabsf(o[9]) + fabsf(o[10]) + fabsf(o[11]);
    const bool bad = !(l < 0x1p100f) || !(chk < INFINITY);
    if (__builtin_expect(__any(bad), 0)) {
      o = attn_slow(kv, q1, q2, lane);
      auto r2 = __builtin_amdgcn_permlane32_swap(__float_as_uint(o[8]), __float_as_uint(o[8]), false, false);
      l = __uint_as_float(r2[1]);
    }
  }
  const float rl = 1.0f / l;
  const int tok = qt * 32 + l31;
  char* const op = ws + WS_O + (unsigned)((tok >> 4) * 20480 + (tok & 15) * 16);
#pragma unroll
  for (int g = 0; g < 3; ++g) {
    const int U = head * 5 + 2 * g + hi;
    if (g < 2 || !hi)
      *(u32x2*)(op + (U >> 3) * 1024 + ((U >> 1) & 3) * 256 + (U & 1) * 8) =
          (u32x2){pk_bf16(o[4 * g] * rl, o[4 * g + 1] * rl), pk_bf16(o[4 * g + 2] * rl, o[4 * g + 3] * rl)};
  }
  STAMP(ws, 1, 4);
  STAMP(ws, 1, 5);
  STAMP_OUT(ws, 1);
}

__global__ __launch_bounds__(512, 2) void k3_proj(const char* __restrict__ x, char* __restrict__ ws) {
  __shared__ __attribute__((aligned(1024))) char smem[100 * 1024];
  lchar* const lds = (lchar*)smem;
  const int lane = threadIdx.x & 63, wave = __builtin_amdgcn_readfirstlane(threadIdx.x >> 6);
  const int rt = (blockIdx.x & 7) * 4 + ((blockIdx.x >> 3) & 3), cb = blockIdx.x >> 5;
  if (wave >= 4) {
    const int lw = wave - 4;
    const unsigned lbase = (unsigned)(uintptr_t)lds;
    const char* const src = ws + WS_WO + (size_t)cb * 100 * 1024;
#pragma unroll
    for (int i = 0; i < 25; ++i) dma16(src + (lw + 4 * i) * 1024, lane * 16, lbase + (lw + 4 * i) * 1024);
    WAITV_BARRIER(18);
    WAITV_BARRIER(12);
    WAITV_BARRIER(6);
    WAITV_BARRIER(0);
    if (lw == 0 && blockIdx.x < 224) {
      const float* const p = (const float*)(ws + WS_CSP) + blockIdx.x * 16 + (lane & 15);
      float s = 0.f;
#pragma unroll
      for (int i = 0; i < 5; ++i) s += p[((lane >> 4) + 4 * i) * 3584];
      s += __shfl_xor(s, 16);
      s += __shfl_xor(s, 32);
      if (lane < 16) ((float*)(ws + WS_CS))[blockIdx.x * 16 + lane] = s;
    }
  } else {
    STAMP_DECL;
    STAMP(ws, 2, 0);
    const int t16 = rt * 4 + wave, tok = t16 * 16 + (lane & 15), G = lane >> 4;
    const char* const ap = ws + WS_O + (size_t)t16 * 20480 + lane * 16;
    bf16x8 a[20];
#pragma unroll
    for (int ks = 0; ks < 20; ++ks) a[ks] = *(const bf16x8*)(ap + ks * 1024);
    u32x2 xr[5];
#pragma unroll
    for (int n = 0; n < 5; ++n) xr[n] = *(const u32x2*)(x + (size_t)tok * 1280 + (cb * 80 + n * 16 + G * 4) * 2);
    f32x4 acc[5] = {};
#pragma unroll
    for (int ks = 0; ks < 20; ++ks) {
      if (ks % 5 == 0) BARRIER();
      if (ks == 0) STAMP(ws, 2, 1);
#pragma unroll
      for (int n = 0; n < 5; ++n) acc[n] = MFMA16(*LDSP(const bf16x8, lds + (ks * 5 + n) * 1024 + lane * 16), a[ks], acc[n]);
    }
    STAMP(ws, 2, 2);
    float s = 0.f, q = 0.f;
    char* const hp = ws + WS_H + (size_t)t16 * 20480 + (lane & 15) * 16;
#pragma unroll
    for (int n = 0; n < 5; ++n) {
      const unsigned w0 = pk_bf16(acc[n][0] + bf_lo(xr[n][0]), acc[n][1] + bf_hi(xr[n][0]));
      const unsigned w1 = pk_bf16(acc[n][2] + bf_lo(xr[n][1]), acc[n][3] + bf_hi(xr[n][1]));
      const int U = cb * 20 + n * 4 + G;
      *(u32x2*)(hp + (U >> 3) * 1024 + ((U >> 1) & 3) * 256 + (U & 1) * 8) = (u32x2){w0, w1};
      const float h0 = bf_lo(w0), h1 = bf_hi(w0), h2 = bf_lo(w1), h3 = bf_hi(w1);
      s += (h0 + h1) + (h2 + h3);
      q += (h0 * h0 + h1 * h1) + (h2 * h2 + h3 * h3);
    }
    s += __shfl_xor(s, 16); q += __shfl_xor(q, 16);
    s += __shfl_xor(s, 32); q += __shfl_xor(q, 32);
    if (lane < 16) *(f32x2*)(ws + WS_ST + (size_t)(tok * 8 + cb) * 8) = (f32x2){s, q};
    STAMP(ws, 2, 4);
    STAMP(ws, 2, 5);
    STAMP_OUT(ws, 2);
  }
}

constexpr int K4_STAGE = 44 * 1024;
__global__ __launch_bounds__(512, 2) void k4_gu(char* __restrict__ ws) {
  __shared__ __attribute__((aligned(1024))) char smem[3 * K4_STAGE];
  lchar* const lds = (lchar*)smem;
  const int lane = threadIdx.x & 63, wave = __builtin_amdgcn_readfirstlane(threadIdx.x >> 6);
  const int xcd = blockIdx.x & 7, bi = blockIdx.x >> 3;
  const int rt = (xcd >> 2) * 8 + (bi & 7), cb = (xcd & 3) * 4 + (bi >> 3);
  if (wave >= 4) {
    const int lw = wave - 4;
    const unsigned lbase = (unsigned)(uintptr_t)lds;
    const char* const ha = ws + WS_H + (size_t)(rt * 8 + lw * 2) * 20480;
    const char* const wb = ws + WS_WGU + (size_t)cb * 20 * 14 * 1024 + lw * 7 * 1024;
    auto issue = [&](int s, int slot) {
#pragma unroll
      for (int i = 0; i < 4; ++i) dma16(ha + (i >> 1) * 20480 + (2 * s + (i & 1)) * 1024, lane * 16, lbase + slot + (lw * 4 + i) * 1024);
#pragma unroll
      for (int i = 0; i < 7; ++i) dma16(wb + s * 28 * 1024 + i * 1024, lane * 16, lbase + slot + (16 + lw * 7 + i) * 1024);
    };
    issue(0, 0);
    issue(1, K4_STAGE);
    issue(2, 2 * K4_STAGE);
    WAITV_BARRIER(22);
    int slot = 0;
    for (int s = 1; s < 8; ++s) {
      WAITV_BARRIER(11);
      issue(s + 2, slot);
      slot = (slot == 2 * K4_STAGE) ? 0 : slot + K4_STAGE;
    }
    WAITV_BARRIER(11);
    WAITV_BARRIER(0);
  } else {
    STAMP_DECL;
    STAMP(ws, 3, 0);
    const int wr = wave >> 1, wc = wave & 1, G = lane >> 4;
    float mu[4], rs[4];
#pragma unroll
    for (int m = 0; m < 4; ++m) {
      const f32x4 p = *(const f32x4*)(ws + WS_ST + (size_t)(rt * 128 + wr * 64 + m * 16 + (lane & 15)) * 64 + G * 16);
      float s = p[0] + p[2], q = p[1] + p[3];
      s += __shfl_xor(s, 16); q += __shfl_xor(q, 16);
      s += __shfl_xor(s, 32); q += __shfl_xor(q, 32);
      mu[m] = s * (1.0f / DM);
      rs[m] = __builtin_amdgcn_rsqf(fmaxf(q * (1.0f / DM) - mu[m] * mu[m], 0.f) + LN_EPS);
    }
    f32x4 cs[7];
#pragma unroll
    for (int n = 0; n < 7; ++n) cs[n] = *(const f32x4*)(ws + WS_CS + (size_t)((cb * 14 + wc * 7 + n) * 16 + G * 4) * 4);
    f32x4 acc[7][4] = {};
    bf16x8 af[2][4], bf[2][7];
    const lchar* const ab = lds + wr * 8 * 1024 + lane * 16;
    const lchar* const bb = lds + (16 + wc * 7) * 1024 + lane * 16;
#define K4_READ(buf, slot, kk)                                                                             \
  do {                                                                                                     \
    _Pragma("unroll") for (int m = 0; m < 4; ++m) af[buf][m] = *LDSP(const bf16x8, ab + (slot) + (2 * m + (kk)) * 1024); \
    _Pragma("unroll") for (int n = 0; n < 7; ++n) bf[buf][n] = *LDSP(const bf16x8, bb + (slot) + ((kk) * 14 + n) * 1024); \
  } while (0)
#define K4_MMA(buf)                                                                                        \
  _Pragma("unroll") for (int n = 0; n < 7; ++n) _Pragma("unroll") for (int m = 0; m < 4; ++m) acc[n][m] = MFMA16(bf[buf][n], af[buf][m], acc[n][m])
    BARRIER();
    STAMP(ws, 3, 1);
    K4_READ(0, 0, 0);
    int slot = 0;
    for (int s = 0; s < 10; ++s) {
      const int nslot = (slot == 2 * K4_STAGE) ? 0 : slot + K4_STAGE;
      K4_READ(1, slot, 1);
      K4_MMA(0);
      if (s < 9) {
        BARRIER();
        K4_READ(0, nslot, 0);
      }
      K4_MMA(1);
      slot = nslot;
    }
#undef K4_READ
#undef K4_MMA
    STAMP(ws, 3, 2);
    const int f0 = (cb * 2 + wc) * 56;
    constexpr float C0 = -2.0f * 0.7978845608028654f * 1.4426950408889634f, C1 = C0 * 0.044715f;
    auto geglu = [&](const f32x4& g, const f32x4& u, const f32x4& cg, const f32x4& cu, float m_, float r_) -> u32x2 {
      const float k0 = C0 * r_, k1 = C1 * r_ * r_ * r_, ir = __builtin_amdgcn_rcpf(r_ * r_);
      float o[4];
#pragma unroll
      for (int j = 0; j < 4; ++j) {
        const float gg = g[j] - m_ * cg[j], uu = u[j] - m_ * cu[j];
        const float e = __builtin_amdgcn_exp2f(gg * (k0 + k1 * (gg * gg)));
        o[j] = gg * uu * __builtin_amdgcn_rcpf(ir + e * ir);
      }
      return (u32x2){pk_bf16(o[0], o[1]), pk_bf16(o[2], o[3])};
    };
    char* const ap = ws + WS_A + (size_t)(rt * 8 + wr * 4) * 54 * 1024 + (lane & 15) * 16;
#pragma unroll
    for (int n = 0; n < 3; ++n) {
      const int U = (f0 >> 2) + n * 4 + G;
      const unsigned off = (U >> 3) * 1024 + ((U >> 1) & 3) * 256 + (U & 1) * 8;
#pragma unroll
      for (int m = 0; m < 4; ++m) {
        const u32x2 r = geglu(acc[n][m], acc[n + 3][m], cs[n], cs[n + 3], mu[m], rs[m]);
        if (U < FF / 4) *(u32x2*)(ap + m * 54 * 1024 + off) = r;
      }
    }
    {
      const int hi = lane >> 5, U = (f0 >> 2) + 12 + (G & 1);
      const unsigned off = (U >> 3) * 1024 + ((U >> 1) & 3) * 256 + (U & 1) * 8;
      f32x4 cg, cu;
#pragma unroll
      for (int j = 0; j < 4; ++j) {
        auto r = __builtin_amdgcn_permlane32_swap(__float_as_uint(cs[6][j]), __float_as_uint(cs[6][j]), false, false);
        cg[j] = __uint_as_float(r[0]);
        cu[j] = __uint_as_float(r[1]);
      }
#pragma unroll
      for (int mp = 0; mp < 2; ++mp) {
        f32x4 g, u;
#pragma unroll
        for (int j = 0; j < 4; ++j) {
          auto r = __builtin_amdgcn_permlane32_swap(__float_as_uint(acc[6][2 * mp][j]), __float_as_uint(acc[6][2 * mp + 1][j]), false, false);
          g[j] = __uint_as_float(r[0]);
          u[j] = __uint_as_float(r[1]);
        }
        const u32x2 r = geglu(g, u, cg, cu, hi ? mu[2 * mp + 1] : mu[2 * mp], hi ? rs[2 * mp + 1] : rs[2 * mp]);
        if (U < FF / 4) *(u32x2*)(ap + (2 * mp + hi) * 54 * 1024 + off) = r;
      }
    }
    STAMP(ws, 3, 4);
    STAMP(ws, 3, 5);
    STAMP_OUT(ws, 3);
  }
}

constexpr int K5_STAGE = 30 * 1024;
__global__ __launch_bounds__(448, 2) void k5_down(char* __restrict__ ws, char* __restrict__ out) {
  __shared__ __attribute__((aligned(1024))) char smem[5 * K5_STAGE];
  lchar* const lds = (lchar*)smem;
  const int lane = threadIdx.x & 63, wave = __builtin_amdgcn_readfirstlane(threadIdx.x >> 6);
  const int xcd = blockIdx.x & 7, bi = blockIdx.x >> 3;
  const int rt = (xcd >> 1) * 8 + (bi & 7), cb = (xcd & 1) * 4 + (bi >> 3);
  if (wave >= 4) {
    const int lw = wave - 4;
    const unsigned lbase = (unsigned)(uintptr_t)lds;
    const char* const src = ws + WS_WD + (size_t)cb * 270 * 1024;
    auto issue = [&](int s, int slot) {
#pragma unroll
      for (int i = 0; i < 10; ++i) dma16(src + (s * 30 + lw + 3 * i) * 1024, lane * 16, lbase + slot + (lw + 3 * i) * 1024);
    };
#pragma unroll
    for (int s = 0; s < 5; ++s) issue(s, s * K5_STAGE);
    WAITV_BARRIER(40);
#pragma unroll
    for (int s = 1; s < 5; ++s) {
      WAITV_BARRIER(30);
      issue(s + 4, (s - 1) * K5_STAGE);
    }
    WAITV_BARRIER(30);
    WAITV_BARRIER(20);
    WAITV_BARRIER(10);
    WAITV_BARRIER(0);
  } else {
    STAMP_DECL;
    STAMP(ws, 4, 0);
    const int t16 = rt * 4 + wave, tok = t16 * 16 + (lane & 15), G = lane >> 4;
    const char* const ap = ws + WS_A + (size_t)t16 * 54 * 1024 + lane * 16;
    constexpr int PF = 30;
    bf16x8 a[PF];
#pragma unroll
    for (int ks = 0; ks < PF; ++ks) a[ks] = *(const bf16x8*)(ap + ks * 1024);
    u32x2 hr[5];
#pragma unroll
    for (int n = 0; n < 5; ++n) {
      const int U = cb * 20 + n * 4 + G;
      hr[n] = *(const u32x2*)(ws + WS_H + (size_t)t16 * 20480 + (lane & 15) * 16 + (U >> 3) * 1024 + ((U >> 1) & 3) * 256 + (U & 1) * 8);
    }
    f32x4 acc[5] = {};
#pragma unroll
    for (int ks = 0; ks < 54; ++ks) {
      if (ks % 6 == 0) BARRIER();
      if (ks == 0) STAMP(ws, 4, 1);
      const bf16x8 av = a[ks % PF];
      if (ks + PF < 54) a[ks % PF] = *(const bf16x8*)(ap + (ks + PF) * 1024);
#pragma unroll
      for (int n = 0; n < 5; ++n)
        acc[n] = MFMA16(*LDSP(const bf16x8, lds + ((ks / 6) % 5) * K5_STAGE + ((ks % 6) * 5 + n) * 1024 + lane * 16), av, acc[n]);
    }
    STAMP(ws, 4, 2);
#pragma unroll
    for (int n = 0; n < 5; ++n) {
      const unsigned w0 = pk_bf16(acc[n][0] + bf_lo(hr[n][0]), acc[n][1] + bf_hi(hr[n][0]));
      const unsigned w1 = pk_bf16(acc[n][2] + bf_lo(hr[n][1]), acc[n][3] + bf_hi(hr[n][1]));
      *(u32x2*)(out + (size_t)tok * 1280 + (cb * 80 + n * 16 + G * 4) * 2) = (u32x2){w0, w1};
    }
    STAMP(ws, 4, 4);
    STAMP(ws, 4, 5);
    STAMP_OUT(ws, 4);
  }
}

extern "C" void kernel_launch(void* const* d_in, const int* in_sizes, int n_in, void* d_out, int out_size, void* d_ws, size_t ws_size,
                              hipStream_t stream) {
  const char* x = (const char*)d_in[0];
  const char* wqkv = (const char*)d_in[1];
  const char* wo = (const char*)d_in[2];
  const char* wg = (const char*)d_in[4];
  const char* wu = (const char*)d_in[5];
  const char* wd = (const char*)d_in[6];
  char* ws = (char*)d_ws;
  k1_qkv<<<256, 768, 0, stream>>>(x, wqkv, ws);
  k2_attn<<<256, 512, 0, stream>>>(wo, wg, wu, wd, ws);
  k3_proj<<<256, 512, 0, stream>>>(x, ws);
  k4_gu<<<256, 512, 0, stream>>>(ws);
  k5_down<<<256, 448, 0, stream>>>(ws, (char*)d_out);
}
```

```cpp
#include <hip/hip_runtime.h>
#include <hip/hip_bf16.h>
#include <stdint.h>

typedef __attribute__((ext_vector_type(8))) short bf16x8;
typedef __attribute__((ext_vector_type(4))) short bf16x4;
typedef __attribute__((ext_vector_type(32))) float f32x32;
typedef __attribute__((ext_vector_type(16))) float f32x16;
typedef __attribute__((ext_vector_type(4))) float f32x4;
typedef __attribute__((ext_vector_type(2))) float f32x2;
typedef __attribute__((ext_vector_type(4))) unsigned u32x4;
typedef __attribute__((ext_vector_type(2))) unsigned u32x2;
typedef __attribute__((address_space(3))) char lchar;
#define LDSP(T, p) ((__attribute__((address_space(3))) T*)(p))

#define ROT1 1
#define ROT2 0
#define ROT3 0
#define ROT4 0
#define ROT5 0
#define CODEPF 1
constexpr int SEQ = 2048, DM = 640, FF = 1728;
constexpr float LN_EPS = 1e-5f;
constexpr float QSCALE = 0.22360679774997896f * 1.4426950408889634f;

constexpr int QT_B = 1280;
constexpr int KV_B = 2688;
constexpr size_t WS_Q = 0;
constexpr size_t WS_KV = WS_Q + (size_t)32 * 64 * QT_B;
constexpr size_t WS_O = WS_KV + (size_t)32 * 64 * KV_B;
constexpr size_t WS_H = WS_O + (size_t)128 * 20 * 1024;
constexpr size_t WS_A = WS_H + (size_t)128 * 20 * 1024;
constexpr size_t WS_WO = WS_A + (size_t)128 * 54 * 1024;
constexpr size_t WS_WGU = WS_WO + (size_t)8 * 20 * 5 * 1024;
constexpr size_t WS_WD = WS_WGU + (size_t)16 * 20 * 14 * 1024;
constexpr size_t WS_CSP = WS_WD + (size_t)8 * 54 * 5 * 1024;
constexpr size_t WS_CS = WS_CSP + (size_t)20 * 3584 * 4;
constexpr size_t WS_ST = WS_CS + (size_t)3584 * 4;

#define MFMA32(a, b, c) __builtin_amdgcn_mfma_f32_32x32x16_bf16(a, b, c, 0, 0, 0)
#define MFMA16(a, b, c) __builtin_amdgcn_mfma_f32_16x16x32_bf16(a, b, c, 0, 0, 0)
#define MFMA2B(a, b, c) __builtin_amdgcn_mfma_f32_32x32x4bf16_1k(a, b, c, 0, 0, 0)
#define PIN(x) asm volatile("" : "+v"(x))
#define SBAR() __builtin_amdgcn_sched_barrier(0)
#define BARRIER() asm volatile("s_waitcnt lgkmcnt(0)\n\ts_barrier" ::: "memory")
#define WAITV_BARRIER(N) asm volatile("s_waitcnt vmcnt(%0) lgkmcnt(0)\n\ts_barrier" ::"i"(N) : "memory")

#define STAMP(ws, kid, i)
#define STAMP_DECL
#define STAMP_OUT(ws, kid)

__device__ __forceinline__ void dma16(const void* sbase, unsigned voff, unsigned lds) {
  asm volatile("s_mov_b32 m0, %2\n\ts_nop 0\n\tglobal_load_lds_dwordx4 %0, %1" ::"v"(voff), "s"(sbase), "s"(lds) : "memory");
}
template <int N>
__device__ __forceinline__ void dma_run(const void* sbase, unsigned voff, unsigned lds) {
  if constexpr (N == 2)
    asm volatile("s_mov_b32 m0, %2\n\ts_nop 0\n\tglobal_load_lds_dwordx4 %0, %1\n\tglobal_load_lds_dwordx4 %0, %1 offset:1024" ::"v"(voff), "s"(sbase), "s"(lds) : "memory");
  else if constexpr (N == 3)
    asm volatile("s_mov_b32 m0, %2\n\ts_nop 0\n\tglobal_load_lds_dwordx4 %0, %1\n\tglobal_load_lds_dwordx4 %0, %1 offset:1024\n\t"
                 "global_load_lds_dwordx4 %0, %1 offset:2048" ::"v"(voff), "s"(sbase), "s"(lds) : "memory");
  else
    asm volatile("s_mov_b32 m0, %2\n\ts_nop 0\n\tglobal_load_lds_dwordx4 %0, %1\n\tglobal_load_lds_dwordx4 %0, %1 offset:1024\n\t"
                 "global_load_lds_dwordx4 %0, %1 offset:2048\n\tglobal_load_lds_dwordx4 %0, %1 offset:3072" ::"v"(voff), "s"(sbase), "s"(lds) : "memory");
}
__device__ __forceinline__ unsigned pk_bf16(float lo, float hi) {
  typedef __attribute__((ext_vector_type(2))) __bf16 bf2;
  bf2 r = __builtin_convertvector((f32x2){lo, hi}, bf2);
  return __builtin_bit_cast(unsigned, r);
}
__device__ __forceinline__ float bf_lo(unsigned w) { return __uint_as_float(w << 16); }
__device__ __forceinline__ float bf_hi(unsigned w) { return __uint_as_float(w & 0xffff0000u); }
__device__ __forceinline__ bf16x4 tr_read(const lchar* p) {
  return __builtin_amdgcn_ds_read_tr16_b64_v4i16(LDSP(bf16x4, p));
}
__device__ __forceinline__ bf16x8 cat8(bf16x4 a, bf16x4 b) { return __builtin_shufflevector(a, b, 0, 1, 2, 3, 4, 5, 6, 7); }
__device__ __forceinline__ float swap32_add(float v) {
  auto r = __builtin_amdgcn_permlane32_swap(__float_as_uint(v), __float_as_uint(v), false, false);
  return __uint_as_float(r[0]) + __uint_as_float(r[1]);
}

constexpr int K1_STAT = 3 * 32768;
template <bool ISV>
__device__ __forceinline__ void k1_compute(lchar* lds, int wave, int lane, int rt, int ct, char* ws) {
  STAMP_DECL;
  STAMP(ws, 0, 0);
  const int wr = wave >> 1, wc = wave & 1, l31 = lane & 31, hi = lane >> 5;
  const lchar* xa[4];
  {
    const int row = wr * 64 + l31, g = hi ^ ((row >> 1) & 7);
#pragma unroll
    for (int ks = 0; ks < 4; ++ks) xa[ks] = lds + row * 128 + 16 * ((2 * ks) ^ g);
  }
  const lchar* wa[2];
  {
    const int blk = (lane >> 4) & 1, q = (lane & 15) >> 2, p = lane & 3;
#pragma unroll
    for (int nt = 0; nt < 2; ++nt) wa[nt] = lds + 16384 + (8 * hi + q) * 256 + 16 * (4 * ((wc * 2 + (nt ^ wr)) ^ q) + 2 * blk + (p >> 1)) + 8 * (p & 1);
  }
  f32x16 acc[2][2] = {};
  f32x4 cs = {};
  const short one = ((lane & 15) == ((lane >> 4) & 1)) ? (short)0x3f80 : (short)0;
  const bf16x8 sel = {one, one, one, one, one, one, one, one};
  bf16x8 xf[2][2];
  bf16x4 wl[2][2], wh[2][2];
#define K1_MM(buf, nt, mt) \
  acc[nt][mt] = ISV ? MFMA32(xf[buf][mt], cat8(wl[buf][nt], wh[buf][nt]), acc[nt][mt]) : MFMA32(cat8(wl[buf][nt], wh[buf][nt]), xf[buf][mt], acc[nt][mt])
#define K1_RX(buf, off, ks, mt) xf[buf][mt] = *LDSP(const bf16x8, xa[ks] + (off) + (mt) * 4096)
#define K1_RW(buf, off, ks, nt) do { wl[buf][nt] = tr_read(wa[nt] + (off) + (ks) * 4096); wh[buf][nt] = tr_read(wa[nt] + (off) + (ks) * 4096 + 1024); } while (0)
#define K1_STEP(buf, off, ks)                                             \
  do {                                                                    \
    SBAR(); K1_MM(buf, 0, 0); SBAR();                                     \
    K1_RX(buf ^ 1, off, ks, 0); K1_RW(buf ^ 1, off, ks, 0); SBAR();       \
    K1_MM(buf, 0, 1); SBAR();                                             \
    K1_RX(buf ^ 1, off, ks, 1); K1_RW(buf ^ 1, off, ks, 1); SBAR();       \
    K1_MM(buf, 1, 0); SBAR();                                             \
    cs = MFMA16(sel, cat8(wl[buf][0], wh[buf][0]), cs); SBAR();           \
    K1_MM(buf, 1, 1); SBAR();                                             \
  } while (0)
  BARRIER();
  STAMP(ws, 0, 1);
  K1_RX(0, 0, 0, 0); K1_RW(0, 0, 0, 0); K1_RX(0, 0, 0, 1); K1_RW(0, 0, 0, 1);
  int slot = 0;
#pragma unroll 1
  for (int s = 0; s < 10; ++s) {
    const int nslot = (slot == 2 * 32768) ? 0 : slot + 32768;
    K1_STEP(0, slot, 1);
    K1_STEP(1, slot, 2);
    K1_STEP(0, slot, 3);
    BARRIER();
    K1_STEP(1, nslot, 0);
    slot = nslot;
  }
  STAMP(ws, 0, 2);
  if (lane < 16) {
    *LDSP(float, lds + K1_STAT + 1024 + (wc * 64 + wr * 32 + lane) * 4) = cs[0];
    *LDSP(float, lds + K1_STAT + 1024 + (wc * 64 + wr * 32 + 16 + lane) * 4) = cs[1];
  }
  BARRIER();
  STAMP(ws, 0, 3);
  const lchar* const st = lds + K1_STAT;
  const int sec = ct / 5, cbase = (ct - sec * 5) * 128 + wc * 64;
  if (!ISV) {
    const int tstride = sec == 0 ? QT_B : KV_B;
    char* const base = ws + (sec == 0 ? WS_Q : WS_KV) + (unsigned)((rt * 4 + wr * 2) * tstride);
    const float qs = sec == 0 ? QSCALE : 1.0f;
    const unsigned vmain = hi * tstride + l31 * 16, vrem = hi * 64 * tstride + 1024 + l31 * 8;
    float mu[2], rs[2];
#pragma unroll
    for (int mt = 0; mt < 2; ++mt) {
      mu[mt] = *LDSP(const float, st + (wr * 64 + mt * 32 + l31) * 4);
      rs[mt] = *LDSP(const float, st + 512 + (wr * 64 + mt * 32 + l31) * 4) * qs;
    }
#pragma unroll
    for (int nt = 0; nt < 2; ++nt)
#pragma unroll
      for (int rg = 0; rg < 4; ++rg) {
        const int cl = (nt ^ wr) * 32 + 8 * rg;
        const f32x4 c4 = *LDSP(const f32x4, st + 1024 + (wc * 64 + cl + 4 * hi) * 4);
        unsigned w[2][2];
#pragma unroll
        for (int mt = 0; mt < 2; ++mt) {
          float v[4];
#pragma unroll
          for (int j = 0; j < 4; ++j) v[j] = rs[mt] * (acc[nt][mt][rg * 4 + j] - mu[mt] * c4[j]);
          w[mt][0] = pk_bf16(v[0], v[1]);
          w[mt][1] = pk_bf16(v[2], v[3]);
        }
        const int P = (cbase + cl) >> 3, hp = (P * 205) >> 10, pm = P - hp * 5;
        if (pm == 2) {
          char* const p = base + (unsigned)(hp * 128 * tstride);
          *(u32x2*)(p + vrem) = (u32x2){w[0][0], w[0][1]};
          *(u32x2*)(p + tstride + vrem) = (u32x2){w[1][0], w[1][1]};
        } else {
          char* const p = base + (unsigned)((hp * 2 + (pm > 2)) * 64 * tstride + ((pm == 1 || pm == 4) ? 512 : 0));
          auto r0 = __builtin_amdgcn_permlane32_swap(w[0][0], w[1][0], false, false);
          auto r1 = __builtin_amdgcn_permlane32_swap(w[0][1], w[1][1], false, false);
          *(u32x4*)(p + vmain) = (u32x4){r0[0], r1[0], r0[1], r1[1]};
        }
      }
  } else {
#pragma unroll
    for (int nt = 0; nt < 2; ++nt) {
      const int cc = cbase + (nt ^ wr) * 32 + l31, head = (cc * 3277) >> 16, d = cc - head * 20;
      const float c1 = *LDSP(const float, st + 1024 + (wc * 64 + (nt ^ wr) * 32 + l31) * 4);
      char* const p = ws + WS_KV + (unsigned)((head * 64 + rt * 4 + wr * 2) * KV_B + 1280 + hi * 352 + d * 16);
      const unsigned fill = d == 0 ? 0x3f803f80u : 0u;
#pragma unroll
      for (int mt = 0; mt < 2; ++mt)
#pragma unroll
        for (int hf = 0; hf < 2; ++hf) {
          unsigned w[4];
#pragma unroll
          for (int g = 0; g < 2; ++g) {
            const int row = wr * 64 + mt * 32 + 16 * hf + 8 * g + 4 * hi;
            const f32x4 m4 = *LDSP(const f32x4, st + row * 4), r4 = *LDSP(const f32x4, st + 512 + row * 4);
            float v[4];
#pragma unroll
            for (int j = 0; j < 4; ++j) v[j] = r4[j] * (acc[nt][mt][hf * 8 + g * 4 + j] - m4[j] * c1);
            w[2 * g] = pk_bf16(v[0], v[1]);
            w[2 * g + 1] = pk_bf16(v[2], v[3]);
          }
          *(u32x4*)(p + mt * KV_B + hf * 704) = (u32x4){w[0], w[1], w[2], w[3]};
          if (d < 2) *(u32x4*)(p + mt * KV_B + hf * 704 + 320) = (u32x4){fill, fill, fill, fill};
        }
    }
  }
  STAMP(ws, 0, 4);
  STAMP(ws, 0, 5);
  STAMP_OUT(ws, 0);
#undef K1_MM
#undef K1_RX
#undef K1_RW
#undef K1_STEP
}

__global__ __launch_bounds__(768, 3) void k1_qkv(const char* __restrict__ x, const char* __restrict__ w, char* __restrict__ ws) {
  __shared__ __attribute__((aligned(1024))) char smem[K1_STAT + 2048];
  lchar* const lds = (lchar*)smem;
  const int lane = threadIdx.x & 63, wave = __builtin_amdgcn_readfirstlane(threadIdx.x >> 6);
  const int xcd = blockIdx.x & 7, bi = blockIdx.x >> 3;
  const int rt = (xcd >> 1) * 4 + (bi & 3), ct = (xcd & 1) * 8 + (bi >> 2);
  if (ct >= 15) return;
  if (wave >= 8) {
    const int lw = wave - 8;
    const unsigned lbase = (unsigned)(uintptr_t)lds;
    const unsigned xv = (lane >> 3) * 1280 + 16 * ((lane & 7) ^ ((4 * lw + (lane >> 4)) & 7));
    const unsigned wv = (lane >> 4) * 3840 + 16 * ((lane & 15) ^ ((lane >> 4) << 2));
    const char* const xb = x + (size_t)(rt * 128 + lw * 8) * 1280;
    const char* const wb = w + (size_t)(lw * 4) * 3840 + ct * 256;
    int ks = ROT1 ? (ct + (rt * 5 >> 1)) % 10 : 0;
    int slot = 0;
    auto issue = [&]() {
#pragma unroll
      for (int i = 0; i < 4; ++i) {
        dma16(xb + i * 32 * 1280 + ks * 128, xv, lbase + slot + (lw + 4 * i) * 1024);
        dma16(wb + (size_t)(ks * 64 + i * 16) * 3840, wv, lbase + slot + 16384 + (lw + 4 * i) * 1024);
      }
      ks = ks == 9 ? 0 : ks + 1;
      slot = (slot == 2 * 32768) ? 0 : slot + 32768;
    };
    STAMP_DECL;
    STAMP(ws, 5, 0);
    issue();
    STAMP(ws, 5, 1);
#pragma unroll 1
    for (int s = 0; s < 9; ++s) {
      issue();
      if (s == 0) STAMP(ws, 5, 2);
      WAITV_BARRIER(8);
      if (s == 0) STAMP(ws, 5, 3);
      if (s == 3) STAMP(ws, 5, 4);
    }
    WAITV_BARRIER(0);
    STAMP(ws, 5, 5);
    STAMP_OUT(ws, 5);
    BARRIER();
    BARRIER();
  } else if (wave >= 4) {
    {
      unsigned long long c0, c1;
      asm volatile("s_getpc_b64 s[20:21]\n\ts_add_u32 s20, s20, _Z6k1_qkvPKcS0_Pc@rel32@lo+4\n\ts_addc_u32 s21, s21, _Z6k1_qkvPKcS0_Pc@rel32@hi+12" : "={s[20:21]}"(c0)::"scc");
      asm volatile("s_getpc_b64 s[22:23]\n\ts_add_u32 s22, s22, _Z5k4_guPc@rel32@lo+4\n\ts_addc_u32 s23, s23, _Z5k4_guPc@rel32@hi+12" : "={s[22:23]}"(c1)::"scc");
      const unsigned len = (unsigned)(c1 - c0) + 6000;
      for (unsigned o = (wave - 4) * 8192; o < len; o += 32768)
        asm volatile("s_mov_b32 m0, %2\n\ts_nop 0\n\tglobal_load_lds_dword %0, %1" ::"v"(min(o + lane * 128, len)), "s"(c0), "s"((unsigned)(uintptr_t)lds + K1_STAT + 1536) : "memory");
    }
    const int row = (wave - 4) * 32 + (lane & 31), hi = lane >> 5;
    const unsigned ra = row * 128, rx = (4 * hi) ^ ((row >> 1) & 7);
    float s0 = 0.f, s1 = 0.f, q0 = 0.f, q1 = 0.f;
    int slot = 0;
    BARRIER();
#pragma unroll 1
    for (int s = 0; s < 10; ++s) {
      u32x4 v[4];
#pragma unroll
      for (int i = 0; i < 4; ++i) v[i] = *LDSP(const u32x4, lds + slot + ra + 16 * (i ^ rx));
#pragma unroll
      for (int i = 0; i < 4; ++i)
#pragma unroll
        for (int j = 0; j < 4; ++j) {
          const float a = bf_lo(v[i][j]), b = bf_hi(v[i][j]);
          asm volatile("v_add_f32 %0, %0, %1" : "+v"(s0) : "v"(a));
          asm volatile("v_add_f32 %0, %0, %1" : "+v"(s1) : "v"(b));
          asm volatile("v_fmac_f32 %0, %1, %1" : "+v"(q0) : "v"(a));
          asm volatile("v_fmac_f32 %0, %1, %1" : "+v"(q1) : "v"(b));
        }
      BARRIER();
      slot = (slot == 2 * 32768) ? 0 : slot + 32768;
    }
    const float sum = swap32_add(s0 + s1), sq = swap32_add(q0 + q1);
    const float mu = sum * (1.0f / DM), var = fmaxf(sq * (1.0f / DM) - mu * mu, 0.f);
    if (hi == 0) {
      *LDSP(float, lds + K1_STAT + row * 4) = mu;
      *LDSP(float, lds + K1_STAT + 512 + row * 4) = __builtin_amdgcn_rsqf(var + LN_EPS);
    }
    BARRIER();
  } else {
    if (ct >= 10) k1_compute<true>(lds, wave, lane, rt, ct, ws);
    else k1_compute<false>(lds, wave, lane, rt, ct, ws);
  }
}

constexpr int CHUNK_B = 8 * KV_B;
constexpr int K2_SCR = 3 * CHUNK_B;

__device__ __forceinline__ float max16(const f32x16& s) {
  float a = fmaxf(fmaxf(s[0], s[1]), s[2]), b = fmaxf(fmaxf(s[3], s[4]), s[5]);
  a = fmaxf(fmaxf(a, s[6]), s[7]); b = fmaxf(fmaxf(b, s[8]), s[9]);
  a = fmaxf(fmaxf(a, s[10]), s[11]); b = fmaxf(fmaxf(b, s[12]), s[13]);
  a = fmaxf(fmaxf(a, s[14]), s[15]);
  a = fmaxf(a, b);
  auto r = __builtin_amdgcn_permlane32_swap(__float_as_uint(a), __float_as_uint(a), false, false);
  return fmaxf(__uint_as_float(r[0]), __uint_as_float(r[1]));
}
__device__ __forceinline__ bf16x8 pack8(const f32x16& s, int o) {
  u32x4 w = {pk_bf16(s[o], s[o + 1]), pk_bf16(s[o + 2], s[o + 3]), pk_bf16(s[o + 4], s[o + 5]), pk_bf16(s[o + 6], s[o + 7])};
  return __builtin_bit_cast(bf16x8, w);
}
__device__ __forceinline__ f32x16 lo16(const f32x32& a) { return __builtin_shufflevector(a, a, 0, 1, 2, 3, 4, 5, 6, 7, 8, 9, 10, 11, 12, 13, 14, 15); }
__device__ __forceinline__ f32x16 hi16(const f32x32& a) { return __builtin_shufflevector(a, a, 16, 17, 18, 19, 20, 21, 22, 23, 24, 25, 26, 27, 28, 29, 30, 31); }

__device__ __forceinline__ void attn_slow(const char* kv, bf16x8 q1a, bf16x8 q1b, bf16x4 q2, int lane, f32x16& oa, f32x16& ob) {
  const int l31 = lane & 31, hi = lane >> 5;
  oa = (f32x16){};
  ob = (f32x16){};
  float ma = -INFINITY, mb = -INFINITY;
#pragma unroll 1
  for (int t = 0; t < 64; ++t) {
    const char* const p = kv + t * KV_B;
    const bf16x8 k1 = *(const bf16x8*)(p + lane * 16);
    const bf16x4 k2 = *(const bf16x4*)(p + 1024 + l31 * 8);
    const int d = l31 > 21 ? 21 : l31;
    const bf16x8 v1 = *(const bf16x8*)(p + 1280 + hi * 352 + d * 16), v2 = *(const bf16x8*)(p + 1280 + 704 + hi * 352 + d * 16);
    const f32x32 s2 = MFMA2B(k2, q2, ((f32x32){}));
    f32x16 sa = MFMA32(k1, q1a, lo16(s2)), sb = MFMA32(k1, q1b, hi16(s2));
    const float na = fmaxf(ma, max16(sa)), nb = fmaxf(mb, max16(sb));
    const float fa = __builtin_amdgcn_exp2f(ma - na), fb = __builtin_amdgcn_exp2f(mb - nb);
    ma = na;
    mb = nb;
#pragma unroll
    for (int r = 0; r < 16; ++r) {
      sa[r] = __builtin_amdgcn_exp2f(sa[r] - na); oa[r] *= fa;
      sb[r] = __builtin_amdgcn_exp2f(sb[r] - nb); ob[r] *= fb;
    }
    oa = MFMA32(v1, pack8(sa, 0), oa);
    oa = MFMA32(v2, pack8(sa, 8), oa);
    ob = MFMA32(v1, pack8(sb, 0), ob);
    ob = MFMA32(v2, pack8(sb, 8), ob);
  }
}
__device__ __forceinline__ float row_sum(const f32x16& o) {
  auto r = __builtin_amdgcn_permlane32_swap(__float_as_uint(o[8]), __float_as_uint(o[8]), false, false);
  return __uint_as_float(r[1]);
}
__device__ __forceinline__ bool unsafe_rows(const f32x16& o) {
  float chk = 0.f;
#pragma unroll
  for (int i = 0; i < 12; ++i) chk += fabsf(o[i]);
  return !(row_sum(o) < 0x1p100f) || !(chk < INFINITY);
}

__global__ __launch_bounds__(512, 2) void k2_attn(const char* __restrict__ wo, const char* __restrict__ wg, const char* __restrict__ wu,
                                                  const char* __restrict__ wd, char* __restrict__ ws) {
  __shared__ __attribute__((aligned(1024))) char smem[K2_SCR + 4 * 8192];
  lchar* const lds = (lchar*)smem;
  const int lane = threadIdx.x & 63, wave = __builtin_amdgcn_readfirstlane(threadIdx.x >> 6);
  const int head = (blockIdx.x & 7) * 4 + (blockIdx.x >> 6), qblk = (blockIdx.x >> 3) & 7;
  const char* const kv = ws + WS_KV + (size_t)head * 64 * KV_B;
  if (wave >= 4) {
    const int lw = wave - 4;
    const unsigned lbase = (unsigned)(uintptr_t)lds;
    int ch = ROT2 ? qblk : 0, slot = 0;
    auto issue = [&]() {
      if (lw < 3) {
        dma_run<4>(kv + ch * CHUNK_B + lw * 7168, lane * 16, lbase + slot + lw * 7168);
        dma_run<3>(kv + ch * CHUNK_B + lw * 7168 + 4096, lane * 16, lbase + slot + lw * 7168 + 4096);
      }
      ch = (ch + 1) & 7;
      slot = (slot == 2 * CHUNK_B) ? 0 : slot + CHUNK_B;
    };
    STAMP_DECL;
    STAMP(ws, 6, 0);
    issue();
    STAMP(ws, 6, 1);
    issue();
    STAMP(ws, 6, 2);
    WAITV_BARRIER(7);
    STAMP(ws, 6, 3);
    issue();
    const int unit = blockIdx.x * 4 + lw;
    const bool gu = unit < 620;
    int uks, uc;
    {
      const char *s0, *s1;
      unsigned voff;
      int pitch;
      if (gu) {
        uks = unit / 31; uc = unit - uks * 31; pitch = FF * 2;
        s0 = wg + uc * 112; s1 = wu + uc * 112;
        voff = (lane >> 3) * pitch + min(lane & 7, uc == 30 ? 5 : 6) * 16;
      } else {
        const int u = unit < 890 ? unit - 620 : unit < 990 ? unit - 890 : 0;
        uks = u / 5; uc = u - uks * 5; pitch = DM * 2;
        s0 = (unit < 890 ? wd : wo) + uc * 256; s1 = s0 + 128;
        voff = (lane >> 3) * pitch + (lane & 7) * 16;
      }
#pragma unroll
      for (int i = 0; i < 4; ++i) {
        dma16(s0 + (size_t)(uks * 32 + i * 8) * pitch, voff, lbase + K2_SCR + lw * 8192 + i * 1024);
        dma16(s1 + (size_t)(uks * 32 + i * 8) * pitch, voff, lbase + K2_SCR + lw * 8192 + 4096 + i * 1024);
      }
    }
    STAMP(ws, 6, 4);
    WAITV_BARRIER(15);
    STAMP(ws, 6, 5);
    WAITV_BARRIER(8);
    issue();
    WAITV_BARRIER(0);
    issue();
    {
      const int p = lane & 3;
      const lchar* const sp = lds + K2_SCR + lw * 8192 + (8 * (lane >> 4) + ((lane & 15) >> 2)) * 128 + p * 8;
      if (gu) {
        const bf16x8 ones = {0x3f80, 0x3f80, 0x3f80, 0x3f80, 0x3f80, 0x3f80, 0x3f80, 0x3f80};
        char* const dst = ws + WS_WGU + (size_t)((((uc >> 1) * 20 + uks) * 14 + (uc & 1) * 7) * 1024);
        float c0 = 0.f, c1 = 0.f;
#pragma unroll
        for (int t = 0; t < 7; ++t) {
          const lchar* const q = t < 3 ? sp + t * 32 : t < 6 ? sp + 4096 + (t - 3) * 32 : sp + 96 + (p >> 1) * (4096 - 16);
          const bf16x8 fr = cat8(tr_read(q), tr_read(q + 512));
          *(bf16x8*)(dst + t * 1024 + lane * 16) = fr;
          const f32x4 cs = MFMA16(ones, fr, ((f32x4){0.f, 0.f, 0.f, 0.f}));
          if (t < 4) c0 = ((lane >> 4) == t) ? cs[0] : c0;
          else c1 = ((lane >> 4) == t - 4) ? cs[0] : c1;
        }
        float* const cp = (float*)(ws + WS_CSP) + uks * 3584 + uc * 112;
        cp[lane] = c0;
        if (lane < 48) cp[64 + lane] = c1;
      } else if (unit < 990) {
        char* const dst = ws + (unit < 890 ? WS_WD : WS_WO);
        const int nks = unit < 890 ? 54 : 20;
#pragma unroll
        for (int t = 0; t < 8; ++t) {
          const lchar* const q = sp + (t >> 2) * 4096 + (t & 3) * 32;
          const bf16x8 fr = cat8(tr_read(q), tr_read(q + 512));
          const int t16 = uc * 8 + t, cb = (t16 * 205) >> 10, tile = t16 - cb * 5;
          *(bf16x8*)(dst + (unsigned)(((cb * nks + uks) * 5 + tile) * 1024) + lane * 16) = fr;
        }
      }
    }
#pragma unroll 1
    for (int c = 3; c < 6; ++c) {
      WAITV_BARRIER(0);
      issue();
    }
    WAITV_BARRIER(0);
    STAMP_OUT(ws, 6);
    return;
  }
  STAMP_DECL;
  STAMP(ws, 1, 0);
  const int l31 = lane & 31, hi = lane >> 5;
  const int qt = qblk * 8 + wave * 2;
  const char* const qp = ws + WS_Q + (size_t)(head * 64 + qt) * QT_B;
  const bf16x8 q1a = *(const bf16x8*)(qp + lane * 16), q1b = *(const bf16x8*)(qp + QT_B + lane * 16);
  const bf16x4 q2 = *(const bf16x4*)(qp + hi * QT_B + 1024 + l31 * 8);
  const lchar* ka = lds + lane * 16;
  const lchar* kb = lds + 1024 + l31 * 8;
  const lchar* va = lds + 1280 + hi * 352 + (l31 > 21 ? 21 : l31) * 16;
  BARRIER();
  STAMP(ws, 1, 1);
  bf16x8 k1, v1, v2 = {0, 0, 0, 0, 0, 0, 0, 0};
  bf16x4 k2;
  u32x4 pa, pb = {0, 0, 0, 0};
  f32x32 negm;
  f32x16 sa[2], sb[2], oa = {}, ob = {};
  {
    k2 = *LDSP(const bf16x4, kb);
    k1 = *LDSP(const bf16x8, ka);
    v1 = *LDSP(const bf16x8, va);
    const f32x32 s2 = MFMA2B(k2, q2, ((f32x32){}));
    sa[0] = MFMA32(k1, q1a, lo16(s2));
    sb[0] = MFMA32(k1, q1b, hi16(s2));
    k2 = *LDSP(const bf16x4, kb + KV_B);
    k1 = *LDSP(const bf16x8, ka + KV_B);
    const float ma = -max16(sa[0]), mb = -max16(sb[0]);
#pragma unroll
    for (int r = 0; r < 16; ++r) { negm[r] = ma; negm[16 + r] = mb; sa[0][r] += ma; sb[0][r] += mb; }
  }
#define EX(S, r) S[r] = __builtin_amdgcn_exp2f(S[r])
#define G1(S) do { EX(S, 0); EX(S, 1); EX(S, 2); EX(S, 3); EX(S, 4); PIN(S); SBAR(); } while (0)
#define G2(S, P) do { EX(S, 5); SBAR(); P[0] = pk_bf16(S[0], S[1]); SBAR(); EX(S, 6); SBAR(); P[1] = pk_bf16(S[2], S[3]); SBAR(); \
                      EX(S, 7); SBAR(); P[2] = pk_bf16(S[4], S[5]); SBAR(); P[3] = pk_bf16(S[6], S[7]); PIN(P); SBAR(); } while (0)
#define G3(S) do { EX(S, 8); EX(S, 9); EX(S, 10); EX(S, 11); EX(S, 12); PIN(S); SBAR(); } while (0)
#define G4(S, P) do { EX(S, 13); SBAR(); P[0] = pk_bf16(S[8], S[9]); SBAR(); EX(S, 14); SBAR(); P[1] = pk_bf16(S[10], S[11]); SBAR(); \
                      EX(S, 15); SBAR(); P[2] = pk_bf16(S[12], S[13]); SBAR(); P[3] = pk_bf16(S[14], S[15]); PIN(P); SBAR(); } while (0)
#define BC(x) __builtin_bit_cast(bf16x8, x)
  int slot = 0;
#pragma unroll 1
  for (int c = 0; c < 8; ++c) {
    const int adv = (slot == 2) ? -2 * CHUNK_B : CHUNK_B;
    slot = (slot == 2) ? 0 : slot + 1;
#pragma unroll
    for (int j = 0; j < 8; ++j) {
      const int cur = j & 1, nxt = cur ^ 1;
      if (j == 6) {
        if (c < 7) asm volatile("s_barrier" ::: "memory");
        ka += adv;
        kb += adv;
      }
      SBAR();
      G1(sa[cur]);
      { const f32x32 t = MFMA2B(k2, q2, negm); sa[nxt] = lo16(t); sb[nxt] = hi16(t); }
      SBAR();
      k2 = *LDSP(const bf16x4, kb + ((j + 2) & 7) * KV_B); SBAR();
      G2(sa[cur], pa);
      G3(sa[cur]);
      ob = MFMA32(v2, BC(pb), ob); SBAR();
      v2 = *LDSP(const bf16x8, va + j * KV_B + 704); SBAR();
      G4(sa[cur], pb);
      oa = MFMA32(v1, BC(pa), oa); SBAR();
      G1(sb[cur]);
      sa[nxt] = MFMA32(k1, q1a, sa[nxt]); SBAR();
      G2(sb[cur], pa);
      sb[nxt] = MFMA32(k1, q1b, sb[nxt]); SBAR();
      k1 = *LDSP(const bf16x8, ka + ((j + 2) & 7) * KV_B); SBAR();
      G3(sb[cur]);
      oa = MFMA32(v2, BC(pb), oa); SBAR();
      G4(sb[cur], pb);
      ob = MFMA32(v1, BC(pa), ob); SBAR();
      if (j == 7) va += adv;
      v1 = *LDSP(const bf16x8, va + ((j + 1) & 7) * KV_B); SBAR();
    }
  }
  ob = MFMA32(v2, BC(pb), ob);
#undef EX
#undef G1
#undef G2
#undef G3
#undef G4
#undef BC
  STAMP(ws, 1, 2);
  if (__builtin_expect(__any(unsafe_rows(oa) || unsafe_rows(ob)), 0)) attn_slow(kv, q1a, q1b, q2, lane, oa, ob);
#pragma unroll
  for (int i = 0; i < 2; ++i) {
    const f32x16& o = i ? ob : oa;
    const float rl = 1.0f / row_sum(o);
    const int tok = (qt + i) * 32 + l31;
    char* const op = ws + WS_O + (unsigned)((tok >> 4) * 20480 + (tok & 15) * 16);
#pragma unroll
    for (int g = 0; g < 3; ++g) {
      const int U = head * 5 + 2 * g + hi;
      if (g < 2 || !hi)
        *(u32x2*)(op + (U >> 3) * 1024 + ((U >> 1) & 3) * 256 + (U & 1) * 8) =
            (u32x2){pk_bf16(o[4 * g] * rl, o[4 * g + 1] * rl), pk_bf16(o[4 * g + 2] * rl, o[4 * g + 3] * rl)};
    }
  }
  STAMP(ws, 1, 4);
  STAMP(ws, 1, 5);
  STAMP_OUT(ws, 1);
}

constexpr int PJ_STAGE = 18 * 1024, PJ_XCH = 4 * PJ_STAGE;
template <bool IS_K3>
__device__ __forceinline__ void proj_body(lchar* lds, const char* __restrict__ x, char* __restrict__ ws, char* __restrict__ out, int rt, int cb, int rot) {
  constexpr int KS = IS_K3 ? 20 : 54, NST = KS / 2;
  const int lane = threadIdx.x & 63, wave = __builtin_amdgcn_readfirstlane(threadIdx.x >> 6);
  if (wave >= 4) {
    const int lw = wave - 4;
    if (lw == 3) {
      if (IS_K3 && blockIdx.x < 224) {
        const float* const p = (const float*)(ws + WS_CSP) + blockIdx.x * 16 + (lane & 15);
        float s = 0.f;
#pragma unroll
        for (int i = 0; i < 5; ++i) s += p[((lane >> 4) + 4 * i) * 3584];
        s += __shfl_xor(s, 16);
        s += __shfl_xor(s, 32);
        if (lane < 16) ((float*)(ws + WS_CS))[blockIdx.x * 16 + lane] = s;
      }
      return;
    }
    const unsigned lbase = (unsigned)(uintptr_t)lds;
    const char* const ab = ws + (IS_K3 ? WS_O : WS_A) + (size_t)rt * 4 * KS * 1024;
    const char* const wb = ws + (IS_K3 ? WS_WO : WS_WD) + (size_t)cb * KS * 5 * 1024;
    const char* const p0 = ab + lw * KS * 1024;
    const char* const p1 = lw == 0 ? ab + 3 * KS * 1024 : wb + (lw - 1) * 2048;
    const char* const p2 = wb + (lw + 2) * 2048;
    const int st1 = lw == 0 ? 2048 : 10240;
    int s = rot, slot = 0;
    auto issue = [&]() {
      dma_run<2>(p0 + s * 2048, lane * 16, lbase + slot + lw * 2048);
      dma_run<2>(p1 + s * st1, lane * 16, lbase + slot + (lw + 3) * 2048);
      dma_run<2>(p2 + s * 10240, lane * 16, lbase + slot + (lw + 6) * 2048);
      s = (s == NST - 1) ? 0 : s + 1;
      slot = (slot == 3 * PJ_STAGE) ? 0 : slot + PJ_STAGE;
    };
    STAMP_DECL;
    STAMP(ws, 7, 0);
    issue();
    STAMP(ws, 7, 1);
    issue();
#pragma unroll 1
    for (int i = 0; i < NST - 2; ++i) {
      issue();
      if (i == 0) STAMP(ws, 7, 2);
      WAITV_BARRIER(12);
      if (i == 0) STAMP(ws, 7, 3);
      if (i == NST / 2) STAMP(ws, 7, 4);
    }
    WAITV_BARRIER(6);
    WAITV_BARRIER(0);
    STAMP(ws, 7, 5);
    STAMP_OUT(ws, IS_K3 ? 7 : 9);
    return;
  }
  STAMP_DECL;
  STAMP(ws, IS_K3 ? 2 : 4, 0);
  const int rh = wave & 1, kp = wave >> 1, G = lane >> 4;
  const int t16 = rt * 4 + rh * 2 + kp, tok = t16 * 16 + (lane & 15);
  u32x2 res[5];
#pragma unroll
  for (int n = 0; n < 5; ++n) {
    const int U = cb * 20 + n * 4 + G;
    if (IS_K3) res[n] = *(const u32x2*)(x + (size_t)tok * 1280 + U * 8);
    else res[n] = *(const u32x2*)(ws + WS_H + (size_t)t16 * 20480 + (lane & 15) * 16 + (U >> 3) * 1024 + ((U >> 1) & 3) * 256 + (U & 1) * 8);
  }
  const lchar* const a0 = lds + ((rh * 2 + kp) * 2 + kp) * 1024 + lane * 16;
  const lchar* const a1 = lds + ((rh * 2 + (kp ^ 1)) * 2 + kp) * 1024 + lane * 16;
  const lchar* const bb = lds + (8 + kp * 5) * 1024 + lane * 16;
  f32x4 acc[2][5] = {};
  bf16x8 af[2][2], bf[2][5];
#define PJ_READ(buf, off)                                                                        \
  do {                                                                                           \
    af[buf][0] = *LDSP(const bf16x8, a0 + (off));                                                \
    af[buf][1] = *LDSP(const bf16x8, a1 + (off));                                                \
    _Pragma("unroll") for (int n = 0; n < 5; ++n) bf[buf][n] = *LDSP(const bf16x8, bb + (off) + n * 1024); \
    SBAR();                                                                                      \
  } while (0)
#define PJ_MMA(buf)                                                                              \
  do {                                                                                           \
    _Pragma("unroll") for (int n = 0; n < 5; ++n) _Pragma("unroll") for (int m = 0; m < 2; ++m) acc[m][n] = MFMA16(bf[buf][n], af[buf][m], acc[m][n]); \
    SBAR();                                                                                      \
  } while (0)
  BARRIER();
  STAMP(ws, IS_K3 ? 2 : 4, 1);
  PJ_READ(0, 0);
  int slot = PJ_STAGE;
#pragma unroll 1
  for (int i = 0; i < (NST - 1) / 2; ++i) {
    BARRIER();
    PJ_READ(1, slot);
    PJ_MMA(0);
    slot = (slot == 3 * PJ_STAGE) ? 0 : slot + PJ_STAGE;
    BARRIER();
    PJ_READ(0, slot);
    PJ_MMA(1);
    slot = (slot == 3 * PJ_STAGE) ? 0 : slot + PJ_STAGE;
  }
  if constexpr ((NST & 1) == 0) {
    BARRIER();
    PJ_READ(1, slot);
    PJ_MMA(0);
    PJ_MMA(1);
  } else {
    PJ_MMA(0);
  }
#undef PJ_READ
#undef PJ_MMA
  STAMP(ws, IS_K3 ? 2 : 4, 2);
#pragma unroll
  for (int n = 0; n < 5; ++n) *LDSP(f32x4, lds + PJ_XCH + (wave * 5 + n) * 1024 + lane * 16) = acc[1][n];
  BARRIER();
  float s = 0.f, q = 0.f;
  char* const hp = ws + WS_H + (size_t)t16 * 20480 + (lane & 15) * 16;
#pragma unroll
  for (int n = 0; n < 5; ++n) {
    const f32x4 v = acc[0][n] + *LDSP(const f32x4, lds + PJ_XCH + ((wave ^ 2) * 5 + n) * 1024 + lane * 16);
    const unsigned w0 = pk_bf16(v[0] + bf_lo(res[n][0]), v[1] + bf_hi(res[n][0]));
    const unsigned w1 = pk_bf16(v[2] + bf_lo(res[n][1]), v[3] + bf_hi(res[n][1]));
    const int U = cb * 20 + n * 4 + G;
    if (IS_K3) {
      *(u32x2*)(hp + (U >> 3) * 1024 + ((U >> 1) & 3) * 256 + (U & 1) * 8) = (u32x2){w0, w1};
      const float h0 = bf_lo(w0), h1 = bf_hi(w0), h2 = bf_lo(w1), h3 = bf_hi(w1);
      s += (h0 + h1) + (h2 + h3);
      q += (h0 * h0 + h1 * h1) + (h2 * h2 + h3 * h3);
    } else {
      *(u32x2*)(out + (size_t)tok * 1280 + U * 8) = (u32x2){w0, w1};
    }
  }
  if (IS_K3) {
    s += __shfl_xor(s, 16); q += __shfl_xor(q, 16);
    s += __shfl_xor(s, 32); q += __shfl_xor(q, 32);
    if (lane < 16) *(f32x2*)(ws + WS_ST + (size_t)(tok * 8 + cb) * 8) = (f32x2){s, q};
  }
  STAMP(ws, IS_K3 ? 2 : 4, 4);
  STAMP(ws, IS_K3 ? 2 : 4, 5);
  STAMP_OUT(ws, IS_K3 ? 2 : 4);
}

__global__ __launch_bounds__(512, 2) void k3_proj(const char* __restrict__ x, char* __restrict__ ws) {
  __shared__ __attribute__((aligned(1024))) char smem[PJ_XCH + 20 * 1024];
  const int bi = blockIdx.x >> 3;
  proj_body<true>((lchar*)smem, x, ws, nullptr, (blockIdx.x & 7) * 4 + (bi & 3), bi >> 2, ROT3 ? ((bi >> 2) + ((bi & 3) * 5 >> 1)) % 10 : 0);
}
__global__ __launch_bounds__(512, 2) void k5_down(char* __restrict__ ws, char* __restrict__ out) {
  __shared__ __attribute__((aligned(1024))) char smem[PJ_XCH + 20 * 1024];
  const int xcd = blockIdx.x & 7, bi = blockIdx.x >> 3;
  proj_body<false>((lchar*)smem, nullptr, ws, out, (xcd >> 1) * 8 + (bi & 7), (xcd & 1) * 4 + (bi >> 3), ROT5 ? ((bi & 7) * 27 / 8 + (bi >> 3) * 27 / 4) % 27 : 0);
}

constexpr int K4_STAGE = 44 * 1024;
__global__ __launch_bounds__(512, 2) void k4_gu(char* __restrict__ ws) {
  __shared__ __attribute__((aligned(1024))) char smem[3 * K4_STAGE];
  lchar* const lds = (lchar*)smem;
  const int lane = threadIdx.x & 63, wave = __builtin_amdgcn_readfirstlane(threadIdx.x >> 6);
  const int xcd = blockIdx.x & 7, bi = blockIdx.x >> 3;
  const int rt = (xcd >> 2) * 8 + (bi & 7), cb = (xcd & 3) * 4 + (bi >> 3);
  if (wave >= 4) {
    const int lw = wave - 4;
    const unsigned lbase = (unsigned)(uintptr_t)lds;
    const char* const ha = ws + WS_H + (size_t)(rt * 8 + lw * 2) * 20480;
    const char* const wb = ws + WS_WGU + (size_t)cb * 20 * 14 * 1024 + lw * 7 * 1024;
    int s = ROT4 ? ((bi & 7) + ((bi >> 3) * 5 >> 1)) % 10 : 0, slot = 0;
    auto issue = [&]() {
      dma_run<2>(ha + s * 2048, lane * 16, lbase + slot + lw * 4096);
      dma_run<2>(ha + 20480 + s * 2048, lane * 16, lbase + slot + lw * 4096 + 2048);
      dma_run<4>(wb + s * 28672, lane * 16, lbase + slot + (16 + lw * 7) * 1024);
      dma_run<3>(wb + s * 28672 + 4096, lane * 16, lbase + slot + (20 + lw * 7) * 1024);
      s = s == 9 ? 0 : s + 1;
      slot = (slot == 2 * K4_STAGE) ? 0 : slot + K4_STAGE;
    };
    STAMP_DECL;
    STAMP(ws, 8, 0);
    issue();
    STAMP(ws, 8, 1);
#pragma unroll 1
    for (int i = 0; i < 9; ++i) {
      issue();
      if (i == 0) STAMP(ws, 8, 2);
      WAITV_BARRIER(11);
      if (i == 0) STAMP(ws, 8, 3);
      if (i == 3) STAMP(ws, 8, 4);
    }
    WAITV_BARRIER(0);
    STAMP(ws, 8, 5);
    STAMP_OUT(ws, 8);
    return;
  }
  STAMP_DECL;
  STAMP(ws, 3, 0);
  const int wr = wave >> 1, wc = wave & 1, G = lane >> 4;
  float mu[4], rs[4];
#pragma unroll
  for (int m = 0; m < 4; ++m) {
    const f32x4 p = *(const f32x4*)(ws + WS_ST + (size_t)(rt * 128 + wr * 64 + m * 16 + (lane & 15)) * 64 + G * 16);
    float s = p[0] + p[2], q = p[1] + p[3];
    s += __shfl_xor(s, 16); q += __shfl_xor(q, 16);
    s += __shfl_xor(s, 32); q += __shfl_xor(q, 32);
    mu[m] = s * (1.0f / DM);
    rs[m] = __builtin_amdgcn_rsqf(fmaxf(q * (1.0f / DM) - mu[m] * mu[m], 0.f) + LN_EPS);
  }
  f32x4 cs[7];
#pragma unroll
  for (int n = 0; n < 7; ++n) cs[n] = *(const f32x4*)(ws + WS_CS + (size_t)((cb * 14 + wc * 7 + n) * 16 + G * 4) * 4);
  f32x4 acc[7][4] = {};
  bf16x8 af[2][4], bf[7];
  const lchar* const ab = lds + wr * 8 * 1024 + lane * 16;
  const lchar* const bb = lds + (16 + wc * 7) * 1024 + lane * 16;
#define K4_STEP(buf, off, kk)                                                                    \
  _Pragma("unroll") for (int n = 0; n < 7; ++n) {                                                \
    SBAR();                                                                                      \
    acc[n][0] = MFMA16(bf[n], af[buf][0], acc[n][0]);                                            \
    acc[n][1] = MFMA16(bf[n], af[buf][1], acc[n][1]);                                            \
    SBAR();                                                                                      \
    if (n < 4) af[buf ^ 1][n] = *LDSP(const bf16x8, ab + (off) + (2 * n + (kk)) * 1024);         \
    SBAR();                                                                                      \
    acc[n][2] = MFMA16(bf[n], af[buf][2], acc[n][2]);                                            \
    acc[n][3] = MFMA16(bf[n], af[buf][3], acc[n][3]);                                            \
    SBAR();                                                                                      \
    bf[n] = *LDSP(const bf16x8, bb + (off) + ((kk) * 14 + n) * 1024);                            \
    SBAR();                                                                                      \
  }
  BARRIER();
  STAMP(ws, 3, 1);
#pragma unroll
  for (int m = 0; m < 4; ++m) af[0][m] = *LDSP(const bf16x8, ab + 2 * m * 1024);
#pragma unroll
  for (int n = 0; n < 7; ++n) bf[n] = *LDSP(const bf16x8, bb + n * 1024);
  int slot = 0;
#pragma unroll 1
  for (int s = 0; s < 10; ++s) {
    K4_STEP(0, slot, 1);
    slot = (slot == 2 * K4_STAGE) ? 0 : slot + K4_STAGE;
    if (s < 9) BARRIER();
    K4_STEP(1, slot, 0);
  }
#undef K4_STEP
  STAMP(ws, 3, 2);
  const int f0 = (cb * 2 + wc) * 56;
  constexpr float C0 = -2.0f * 0.7978845608028654f * 1.4426950408889634f, C1 = C0 * 0.044715f;
  auto geglu = [&](const f32x4& g, const f32x4& u, const f32x4& cg, const f32x4& cu, float m_, float r_) -> u32x2 {
    const float k0 = C0 * r_, k1 = C1 * r_ * r_ * r_, ir = __builtin_amdgcn_rcpf(r_ * r_);
    const f32x4 gg = g - m_ * cg, uu = u - m_ * cu;
    const f32x4 z = gg * (k0 + k1 * (gg * gg));
    f32x4 e, r;
#pragma unroll
    for (int j = 0; j < 4; ++j) e[j] = __builtin_amdgcn_exp2f(z[j]);
    e = ir + e * ir;
#pragma unroll
    for (int j = 0; j < 4; ++j) r[j] = __builtin_amdgcn_rcpf(e[j]);
    const f32x4 o = gg * uu * r;
    return (u32x2){pk_bf16(o[0], o[1]), pk_bf16(o[2], o[3])};
  };
  char* const ap = ws + WS_A + (size_t)(rt * 8 + wr * 4) * 54 * 1024 + (lane & 15) * 16;
#pragma unroll
  for (int n = 0; n < 3; ++n) {
    const int U = (f0 >> 2) + n * 4 + G;
    const unsigned off = (U >> 3) * 1024 + ((U >> 1) & 3) * 256 + (U & 1) * 8;
#pragma unroll
    for (int m = 0; m < 4; ++m) {
      const u32x2 r = geglu(acc[n][m], acc[n + 3][m], cs[n], cs[n + 3], mu[m], rs[m]);
      if (f0 < FF) *(u32x2*)(ap + m * 54 * 1024 + off) = r;
    }
  }
  {
    const int hi = lane >> 5, U = (f0 >> 2) + 12 + (G & 1);
    const unsigned off = (U >> 3) * 1024 + ((U >> 1) & 3) * 256 + (U & 1) * 8;
    f32x4 cg, cu;
#pragma unroll
    for (int j = 0; j < 4; ++j) {
      auto r = __builtin_amdgcn_permlane32_swap(__float_as_uint(cs[6][j]), __float_as_uint(cs[6][j]), false, false);
      cg[j] = __uint_as_float(r[0]);
      cu[j] = __uint_as_float(r[1]);
    }
#pragma unroll
    for (int mp = 0; mp < 2; ++mp) {
      f32x4 g, u;
#pragma unroll
      for (int j = 0; j < 4; ++j) {
        auto r = __builtin_amdgcn_permlane32_swap(__float_as_uint(acc[6][2 * mp][j]), __float_as_uint(acc[6][2 * mp + 1][j]), false, false);
        g[j] = __uint_as_float(r[0]);
        u[j] = __uint_as_float(r[1]);
      }
      const u32x2 r = geglu(g, u, cg, cu, hi ? mu[2 * mp + 1] : mu[2 * mp], hi ? rs[2 * mp + 1] : rs[2 * mp]);
      if (f0 + 48 < FF) *(u32x2*)(ap + (2 * mp + hi) * 54 * 1024 + off) = r;
    }
  }
  STAMP(ws, 3, 4);
  STAMP(ws, 3, 5);
  STAMP_OUT(ws, 3);
}

extern "C" void kernel_launch(void* const* d_in, const int* in_sizes, int n_in, void* d_out, int out_size, void* d_ws, size_t ws_size,
                              hipStream_t stream) {
  const char* x = (const char*)d_in[0];
  const char* wqkv = (const char*)d_in[1];
  const char* wo = (const char*)d_in[2];
  const char* wg = (const char*)d_in[4];
  const char* wu = (const char*)d_in[5];
  const char* wd = (const char*)d_in[6];
  char* ws = (char*)d_ws;
  k1_qkv<<<256, 768, 0, stream>>>(x, wqkv, ws);
  k2_attn<<<256, 512, 0, stream>>>(wo, wg, wu, wd, ws);
  k3_proj<<<256, 512, 0, stream>>>(x, ws);
  k4_gu<<<256, 512, 0, stream>>>(ws);
  k5_down<<<256, 512, 0, stream>>>(ws, (char*)d_out);
}
```

```cpp
#include <hip/hip_runtime.h>
#include <hip/hip_bf16.h>
#include <stdint.h>

typedef __attribute__((ext_vector_type(8))) short bf16x8;
typedef __attribute__((ext_vector_type(4))) short bf16x4;
typedef __attribute__((ext_vector_type(32))) float f32x32;
typedef __attribute__((ext_vector_type(16))) float f32x16;
typedef __attribute__((ext_vector_type(4))) float f32x4;
typedef __attribute__((ext_vector_type(2))) float f32x2;
typedef __attribute__((ext_vector_type(4))) unsigned u32x4;
typedef __attribute__((ext_vector_type(2))) unsigned u32x2;
typedef __attribute__((address_space(3))) char lchar;
#define LDSP(T, p) ((__attribute__((address_space(3))) T*)(p))

constexpr int DM = 640, FF = 1728;
constexpr float LN_EPS = 1e-5f;
constexpr float QSCALE = 0.22360679774997896f * 1.4426950408889634f;

constexpr int QP_B = 2560, KVP_B = 5376;
constexpr int WS_Q = 0;
constexpr int WS_KV = WS_Q + 32 * 32 * QP_B;
constexpr int WS_O = WS_KV + 32 * 32 * KVP_B;
constexpr int WS_H = WS_O + 32 * 32 * QP_B;
constexpr int WS_A = WS_H + 128 * 20 * 1024;
constexpr int WS_WO = WS_A + 128 * 54 * 1024;
constexpr int WS_WGU = WS_WO + 8 * 20 * 5 * 1024;
constexpr int WS_WD = WS_WGU + 16 * 20 * 14 * 1024;
constexpr int WS_CSP = WS_WD + 8 * 54 * 5 * 1024;
constexpr int WS_ST = WS_CSP + 20 * 3584 * 4;
constexpr int WS_CNT = WS_ST + 8 * 2048 * 8;
constexpr int WS_END = WS_CNT + (3 * 256 + 8) * 128;

#define MFMA32(a, b, c) __builtin_amdgcn_mfma_f32_32x32x16_bf16(a, b, c, 0, 0, 0)
#define MFMA16(a, b, c) __builtin_amdgcn_mfma_f32_16x16x32_bf16(a, b, c, 0, 0, 0)
#define MFMA2B(a, b, c) __builtin_amdgcn_mfma_f32_32x32x4bf16_1k(a, b, c, 0, 0, 0)
#define PIN(x) asm volatile("" : "+v"(x))
#define SBAR() __builtin_amdgcn_sched_barrier(0)
#define BARRIER() asm volatile("s_waitcnt lgkmcnt(0)\n\ts_barrier" ::: "memory")
#define WAITV_BARRIER(N) asm volatile("s_waitcnt vmcnt(%0) lgkmcnt(0)\n\ts_barrier" ::"i"(N) : "memory")
#define DRAIN() asm volatile("s_waitcnt vmcnt(0)" ::: "memory")
#define BC8(x) __builtin_bit_cast(bf16x8, x)
constexpr int SC1 = 16;

#define STAMP(i)
#define STAMP_DECL
#define STAMP_OUT(ws, kid)

__device__ __forceinline__ void dma16(const void* sbase, unsigned voff, unsigned lds) {
  asm volatile("s_mov_b32 m0, %2\n\ts_nop 0\n\tglobal_load_lds_dwordx4 %0, %1" ::"v"(voff), "s"(sbase), "s"(lds) : "memory");
}
template <int N>
__device__ __forceinline__ void dma_run(const void* sbase, unsigned voff, unsigned lds) {
#define GL(o) "\n\tglobal_load_lds_dwordx4 %0, %1 offset:" #o " sc1"
  if constexpr (N == 1) asm volatile("s_mov_b32 m0, %2\n\ts_nop 0" GL(0)::"v"(voff), "s"(sbase), "s"(lds) : "memory");
  else if constexpr (N == 2) asm volatile("s_mov_b32 m0, %2\n\ts_nop 0" GL(0) GL(1024)::"v"(voff), "s"(sbase), "s"(lds) : "memory");
  else if constexpr (N == 3) asm volatile("s_mov_b32 m0, %2\n\ts_nop 0" GL(0) GL(1024) GL(2048)::"v"(voff), "s"(sbase), "s"(lds) : "memory");
  else asm volatile("s_mov_b32 m0, %2\n\ts_nop 0" GL(0) GL(1024) GL(2048) GL(3072)::"v"(voff), "s"(sbase), "s"(lds) : "memory");
#undef GL
}
template <int N>
__device__ __forceinline__ void dma_lin(const char* sbase, unsigned voff, unsigned lds) {
#pragma unroll
  for (int i = 0; i + 4 <= N; i += 4) dma_run<4>(sbase + i * 1024, voff, lds + i * 1024);
  if constexpr (N % 4 != 0) dma_run<N % 4>(sbase + (N / 4 * 4) * 1024, voff, lds + (N / 4 * 4) * 1024);
}
__device__ __forceinline__ void touch64(const void* sbase, unsigned voff, unsigned lds) {
  asm volatile("s_mov_b32 m0, %2\n\ts_nop 0\n\tglobal_load_lds_dword %0, %1 sc1" ::"v"(voff), "s"(sbase), "s"(lds) : "memory");
}
__device__ __forceinline__ unsigned pk_bf16(float lo, float hi) {
  typedef __attribute__((ext_vector_type(2))) __bf16 bf2;
  bf2 r = __builtin_convertvector((f32x2){lo, hi}, bf2);
  return __builtin_bit_cast(unsigned, r);
}
__device__ __forceinline__ float bf_lo(unsigned w) { return __uint_as_float(w << 16); }
__device__ __forceinline__ float bf_hi(unsigned w) { return __uint_as_float(w & 0xffff0000u); }
__device__ __forceinline__ bf16x4 tr_read(const lchar* p) { return __builtin_amdgcn_ds_read_tr16_b64_v4i16(LDSP(bf16x4, p)); }
__device__ __forceinline__ bf16x8 cat8(bf16x4 a, bf16x4 b) { return __builtin_shufflevector(a, b, 0, 1, 2, 3, 4, 5, 6, 7); }
__device__ __forceinline__ float swap32_add(float v) {
  auto r = __builtin_amdgcn_permlane32_swap(__float_as_uint(v), __float_as_uint(v), false, false);
  return __uint_as_float(r[0]) + __uint_as_float(r[1]);
}
__device__ __forceinline__ float swap16_add(float v) {
  auto r = __builtin_amdgcn_permlane16_swap(__float_as_uint(v), __float_as_uint(v), false, false);
  return __uint_as_float(r[0]) + __uint_as_float(r[1]);
}
__device__ __forceinline__ u32x4 widen32(u32x2 a, u32x2 b) {
  auto r0 = __builtin_amdgcn_permlane32_swap(a[0], b[0], false, false);
  auto r1 = __builtin_amdgcn_permlane32_swap(a[1], b[1], false, false);
  return (u32x4){r0[0], r1[0], r0[1], r1[1]};
}
__device__ __forceinline__ u32x4 widen16(u32x2 a, u32x2 b) {
  auto r0 = __builtin_amdgcn_permlane16_swap(a[0], b[0], false, false);
  auto r1 = __builtin_amdgcn_permlane16_swap(a[1], b[1], false, false);
  return (u32x4){r0[0], r1[0], r0[1], r1[1]};
}
template <typename R>
__device__ __forceinline__ void st128(u32x4 v, R rsrc, unsigned voff, int soff, int aux) {
  __builtin_amdgcn_raw_buffer_store_b128(v, rsrc, voff + soff, 0, SC1);
}
__device__ __forceinline__ unsigned* counter(char* ws, int i) { return (unsigned*)(ws + WS_CNT) + i * 32; }
__device__ __forceinline__ void signal_add(unsigned* c) { __hip_atomic_fetch_add(c, 1u, __ATOMIC_RELAXED, __HIP_MEMORY_SCOPE_AGENT); }
__device__ __forceinline__ void wait_count(unsigned* c, unsigned want) {
  for (unsigned spins = 0; spins < (1u << 22); ++spins) {
    if (__all(__hip_atomic_load(c, __ATOMIC_RELAXED, __HIP_MEMORY_SCOPE_AGENT) >= want)) break;
    __builtin_amdgcn_s_sleep(1);
  }
  asm volatile("" ::: "memory");
}

constexpr int K1_STAT = 3 * 32768;
template <bool ISV>
__device__ __forceinline__ void k1_compute(lchar* lds, int wave, int lane, int rt, int ct, char* ws) {
  STAMP_DECL;
  STAMP(0);
  const int wr = wave >> 1, wc = wave & 1, l31 = lane & 31, hi = lane >> 5;
  const lchar* xa[4];
  {
    const int row = wr * 64 + l31, g = hi ^ ((row >> 1) & 7);
#pragma unroll
    for (int ks = 0; ks < 4; ++ks) xa[ks] = lds + row * 128 + 16 * ((2 * ks) ^ g);
  }
  const lchar* wa[2];
  {
    const int blk = (lane >> 4) & 1, q = (lane & 15) >> 2, p = lane & 3;
#pragma unroll
    for (int nt = 0; nt < 2; ++nt) wa[nt] = lds + 16384 + (8 * hi + q) * 256 + 16 * (4 * ((wc * 2 + (nt ^ wr)) ^ q) + 2 * blk + (p >> 1)) + 8 * (p & 1);
  }
  f32x16 acc[2][2] = {};
  f32x4 cs = {};
  const short one = ((lane & 15) == ((lane >> 4) & 1)) ? (short)0x3f80 : (short)0;
  const bf16x8 sel = {one, one, one, one, one, one, one, one};
  bf16x8 xf[2][2];
  bf16x4 wl[2][2], wh[2][2];
#define K1_MM(buf, nt, mt) \
  acc[nt][mt] = ISV ? MFMA32(xf[buf][mt], cat8(wl[buf][nt], wh[buf][nt]), acc[nt][mt]) : MFMA32(cat8(wl[buf][nt], wh[buf][nt]), xf[buf][mt], acc[nt][mt])
#define K1_RX(buf, off, ks, mt) xf[buf][mt] = *LDSP(const bf16x8, xa[ks] + (off) + (mt) * 4096)
#define K1_RW(buf, off, ks, nt) do { wl[buf][nt] = tr_read(wa[nt] + (off) + (ks) * 4096); wh[buf][nt] = tr_read(wa[nt] + (off) + (ks) * 4096 + 1024); } while (0)
#define K1_STEP(buf, off, ks)                                             \
  do {                                                                    \
    SBAR(); K1_MM(buf, 0, 0); SBAR();                                     \
    K1_RX(buf ^ 1, off, ks, 0); K1_RW(buf ^ 1, off, ks, 0); SBAR();       \
    K1_MM(buf, 0, 1); SBAR();                                             \
    K1_RX(buf ^ 1, off, ks, 1); K1_RW(buf ^ 1, off, ks, 1); SBAR();       \
    K1_MM(buf, 1, 0); SBAR();                                             \
    cs = MFMA16(sel, cat8(wl[buf][0], wh[buf][0]), cs); SBAR();           \
    K1_MM(buf, 1, 1); SBAR();                                             \
  } while (0)
  BARRIER();
  STAMP(1);
  K1_RX(0, 0, 0, 0); K1_RW(0, 0, 0, 0); K1_RX(0, 0, 0, 1); K1_RW(0, 0, 0, 1);
  int slot = 0;
#pragma unroll 1
  for (int s = 0; s < 10; ++s) {
    const int nslot = (slot == 2 * 32768) ? 0 : slot + 32768;
    K1_STEP(0, slot, 1);
    K1_STEP(1, slot, 2);
    K1_STEP(0, slot, 3);
    BARRIER();
    K1_STEP(1, nslot, 0);
    slot = nslot;
  }
  STAMP(2);
  if (lane < 16) {
    *LDSP(float, lds + K1_STAT + 1024 + (wc * 64 + wr * 32 + lane) * 4) = cs[0];
    *LDSP(float, lds + K1_STAT + 1024 + (wc * 64 + wr * 32 + 16 + lane) * 4) = cs[1];
  }
  BARRIER();
  STAMP(3);
  const lchar* const st = lds + K1_STAT;
  const int sec = ct / 5, cbase = (ct - sec * 5) * 128 + wc * 64;
  if (!ISV) {
    const int pstride = sec == 0 ? QP_B : KVP_B;
    char* const base = ws + (sec == 0 ? WS_Q : WS_KV) + (unsigned)((rt * 2 + wr) * pstride);
    const float qs = sec == 0 ? QSCALE : 1.0f;
    const unsigned vmain = hi * 1024 + l31 * 16, vrem = hi * 32 * pstride + 2048 + l31 * 16;
    float mu[2], rs[2];
    f32x4 c4[2][4];
#pragma unroll
    for (int mt = 0; mt < 2; ++mt) {
      mu[mt] = *LDSP(const float, st + (wr * 64 + mt * 32 + l31) * 4);
      rs[mt] = *LDSP(const float, st + 512 + (wr * 64 + mt * 32 + l31) * 4) * qs;
    }
#pragma unroll
    for (int nt = 0; nt < 2; ++nt)
#pragma unroll
      for (int rg = 0; rg < 4; ++rg) c4[nt][rg] = *LDSP(const f32x4, st + 1024 + (wc * 64 + (nt ^ wr) * 32 + 8 * rg + 4 * hi) * 4);
#pragma unroll
    for (int nt = 0; nt < 2; ++nt)
#pragma unroll
      for (int rg = 0; rg < 4; ++rg) {
        u32x2 w[2];
#pragma unroll
        for (int mt = 0; mt < 2; ++mt) {
          float v[4];
#pragma unroll
          for (int j = 0; j < 4; ++j) v[j] = rs[mt] * (acc[nt][mt][rg * 4 + j] - mu[mt] * c4[nt][rg][j]);
          w[mt] = (u32x2){pk_bf16(v[0], v[1]), pk_bf16(v[2], v[3])};
        }
        const int P = (cbase + (nt ^ wr) * 32 + 8 * rg) >> 3, hp = (P * 205) >> 10, pm = P - hp * 5;
        const bool rem = pm == 2;
        const u32x4 sw = widen32(w[0], w[1]);
        const u32x4 val = {rem ? w[0][0] : sw[0], rem ? w[0][1] : sw[1], rem ? w[1][0] : sw[2], rem ? w[1][1] : sw[3]};
        char* const p = base + (unsigned)((hp * 2 + (pm > 2)) * 32 * pstride + ((pm == 1 || pm == 4) ? 512 : 0));
        *(u32x4*)(p + (rem ? vrem : vmain)) = val;
      }
  } else {
    f32x4 m4[2][2][2], r4[2][2][2];
#pragma unroll
    for (int mt = 0; mt < 2; ++mt)
#pragma unroll
      for (int hf = 0; hf < 2; ++hf)
#pragma unroll
        for (int g = 0; g < 2; ++g) {
          const int row = wr * 64 + mt * 32 + 16 * hf + 8 * g + 4 * hi;
          m4[mt][hf][g] = *LDSP(const f32x4, st + row * 4);
          r4[mt][hf][g] = *LDSP(const f32x4, st + 512 + row * 4);
        }
#pragma unroll
    for (int nt = 0; nt < 2; ++nt) {
      const int cc = cbase + (nt ^ wr) * 32 + l31, head = (cc * 3277) >> 16, d = cc - head * 20;
      const float c1 = *LDSP(const float, st + 1024 + (wc * 64 + (nt ^ wr) * 32 + l31) * 4);
      char* const p = ws + WS_KV + (unsigned)((head * 32 + rt * 2 + wr) * KVP_B + 2560 + hi * 352 + d * 16);
#pragma unroll
      for (int mt = 0; mt < 2; ++mt)
#pragma unroll
        for (int hf = 0; hf < 2; ++hf) {
          unsigned w[4];
#pragma unroll
          for (int g = 0; g < 2; ++g) {
            float v[4];
#pragma unroll
            for (int j = 0; j < 4; ++j) v[j] = r4[mt][hf][g][j] * (acc[nt][mt][hf * 8 + g * 4 + j] - m4[mt][hf][g][j] * c1);
            w[2 * g] = pk_bf16(v[0], v[1]);
            w[2 * g + 1] = pk_bf16(v[2], v[3]);
          }
          *(u32x4*)(p + mt * 1408 + hf * 704) = (u32x4){w[0], w[1], w[2], w[3]};
        }
    }
  }
  STAMP(4);
  STAMP_OUT(ws, 0);
#undef K1_MM
#undef K1_RX
#undef K1_RW
#undef K1_STEP
}

__global__ __launch_bounds__(768, 3) void k1_qkv(const char* __restrict__ x, const char* __restrict__ w, char* __restrict__ ws) {
  __shared__ __attribute__((aligned(1024))) char smem[K1_STAT + 2048];
  lchar* const lds = (lchar*)smem;
  const int lane = threadIdx.x & 63, wave = __builtin_amdgcn_readfirstlane(threadIdx.x >> 6);
  const int xcd = blockIdx.x & 7, bi = blockIdx.x >> 3;
  const int rt = (xcd >> 1) * 4 + (bi & 3), ct = (xcd & 1) * 8 + (bi >> 2);
  if (wave == 4) {
    if (lane < 3) *counter(ws, lane * 256 + blockIdx.x) = 0;
    if (lane == 3 && blockIdx.x < 8) *counter(ws, 768 + blockIdx.x) = 0;
    const unsigned fill = (lane & 1) ? 0u : 0x3f803f80u;
    *(u32x4*)(ws + WS_KV + (unsigned)((blockIdx.x * 4 + (lane >> 4)) * KVP_B + 2560 + ((lane >> 1) & 7) * 352 + 320 + (lane & 1) * 16)) = (u32x4){fill, fill, fill, fill};
  }
  if (ct >= 15) return;
  if (wave >= 8) {
    const int lw = wave - 8;
    const unsigned lbase = (unsigned)(uintptr_t)lds;
    const unsigned xv = (lane >> 3) * 1280 + 16 * ((lane & 7) ^ ((4 * lw + (lane >> 4)) & 7));
    const unsigned wv = (lane >> 4) * 3840 + 16 * ((lane & 15) ^ ((lane >> 4) << 2));
    const char* const xb = x + (size_t)(rt * 128 + lw * 8) * 1280;
    const char* const wb = w + (size_t)(lw * 4) * 3840 + ct * 256;
    int ks = (ct + (rt * 5 >> 1)) % 10;
    int slot = 0;
    auto issue = [&]() {
#pragma unroll
      for (int i = 0; i < 4; ++i) {
        dma16(xb + i * 32 * 1280 + ks * 128, xv, lbase + slot + (lw + 4 * i) * 1024);
        dma16(wb + (size_t)(ks * 64 + i * 16) * 3840, wv, lbase + slot + 16384 + (lw + 4 * i) * 1024);
      }
      ks = ks == 9 ? 0 : ks + 1;
      slot = (slot == 2 * 32768) ? 0 : slot + 32768;
    };
    issue();
#pragma unroll 1
    for (int s = 0; s < 9; ++s) {
      issue();
      WAITV_BARRIER(8);
    }
    WAITV_BARRIER(0);
  } else if (wave >= 4) {
    {
      unsigned long long c0, c1;
      asm volatile("s_getpc_b64 s[20:21]\n\ts_add_u32 s20, s20, _Z6k1_qkvPKcS0_Pc@rel32@lo+4\n\ts_addc_u32 s21, s21, _Z6k1_qkvPKcS0_Pc@rel32@hi+12" : "={s[20:21]}"(c0)::"scc");
      asm volatile("s_getpc_b64 s[22:23]\n\ts_add_u32 s22, s22, _Z6k3_endv@rel32@lo+4\n\ts_addc_u32 s23, s23, _Z6k3_endv@rel32@hi+12" : "={s[22:23]}"(c1)::"scc");
      const unsigned len = (unsigned)(c1 - c0);
      if (len < 65536)
        for (unsigned o = (wave - 4) * 8192; o < len; o += 32768) touch64((const void*)c0, min(o + lane * 128, len), (unsigned)(uintptr_t)lds + K1_STAT + 1536);
    }
    const int row = (wave - 4) * 32 + (lane & 31), hi = lane >> 5;
    const unsigned ra = row * 128, rx = (4 * hi) ^ ((row >> 1) & 7);
    float s0 = 0.f, s1 = 0.f, q0 = 0.f, q1 = 0.f;
    int slot = 0;
    BARRIER();
#pragma unroll 1
    for (int s = 0; s < 10; ++s) {
      u32x4 v[4];
#pragma unroll
      for (int i = 0; i < 4; ++i) v[i] = *LDSP(const u32x4, lds + slot + ra + 16 * (i ^ rx));
#pragma unroll
      for (int i = 0; i < 4; ++i)
#pragma unroll
        for (int j = 0; j < 4; ++j) {
          const float a = bf_lo(v[i][j]), b = bf_hi(v[i][j]);
          asm volatile("v_add_f32 %0, %0, %4\n\tv_add_f32 %1, %1, %5\n\tv_fmac_f32 %2, %4, %4\n\tv_fmac_f32 %3, %5, %5"
                       : "+v"(s0), "+v"(s1), "+v"(q0), "+v"(q1) : "v"(a), "v"(b));
        }
      BARRIER();
      slot = (slot == 2 * 32768) ? 0 : slot + 32768;
    }
    const float sum = swap32_add(s0 + s1), sq = swap32_add(q0 + q1);
    const float mu = sum * (1.0f / DM), var = fmaxf(sq * (1.0f / DM) - mu * mu, 0.f);
    if (hi == 0) {
      *LDSP(float, lds + K1_STAT + row * 4) = mu;
      *LDSP(float, lds + K1_STAT + 512 + row * 4) = __builtin_amdgcn_rsqf(var + LN_EPS);
    }
    BARRIER();
  } else {
    if (ct >= 10) k1_compute<true>(lds, wave, lane, rt, ct, ws);
    else k1_compute<false>(lds, wave, lane, rt, ct, ws);
  }
}

constexpr int CHUNK_B = 4 * KVP_B;
constexpr int L_SCR = 3 * CHUNK_B;
constexpr int L_WO = 163840 - 100 * 1024;
constexpr int L_B4 = 0, B4_B = 28 * 1024;
constexpr int L_A4 = 3 * B4_B, A4_B = 16 * 1024;
constexpr int L_CS = L_A4 + 3 * A4_B;
constexpr int L_XCH = 24 * 5 * 1024;

__device__ __forceinline__ float max16(const f32x16& s) {
  float a = fmaxf(fmaxf(s[0], s[1]), s[2]), b = fmaxf(fmaxf(s[3], s[4]), s[5]);
  a = fmaxf(fmaxf(a, s[6]), s[7]); b = fmaxf(fmaxf(b, s[8]), s[9]);
  a = fmaxf(fmaxf(a, s[10]), s[11]); b = fmaxf(fmaxf(b, s[12]), s[13]);
  a = fmaxf(fmaxf(a, s[14]), s[15]);
  a = fmaxf(a, b);
  auto r = __builtin_amdgcn_permlane32_swap(__float_as_uint(a), __float_as_uint(a), false, false);
  return fmaxf(__uint_as_float(r[0]), __uint_as_float(r[1]));
}
__device__ __forceinline__ bf16x8 pack8(const f32x16& s, int o) {
  u32x4 w = {pk_bf16(s[o], s[o + 1]), pk_bf16(s[o + 2], s[o + 3]), pk_bf16(s[o + 4], s[o + 5]), pk_bf16(s[o + 6], s[o + 7])};
  return BC8(w);
}
__device__ __forceinline__ f32x16 lo16(const f32x32& a) { return __builtin_shufflevector(a, a, 0, 1, 2, 3, 4, 5, 6, 7, 8, 9, 10, 11, 12, 13, 14, 15); }
__device__ __forceinline__ f32x16 hi16(const f32x32& a) { return __builtin_shufflevector(a, a, 16, 17, 18, 19, 20, 21, 22, 23, 24, 25, 26, 27, 28, 29, 30, 31); }
__device__ __forceinline__ bf16x4 lo4(bf16x8 a) { return __builtin_shufflevector(a, a, 0, 1, 2, 3); }
__device__ __forceinline__ bf16x4 hi4(bf16x8 a) { return __builtin_shufflevector(a, a, 4, 5, 6, 7); }

__device__ __forceinline__ void attn_slow(const char* kv, bf16x8 q1a, bf16x8 q1b, bf16x4 q2, int lane, f32x16& oa, f32x16& ob) {
  const int l31 = lane & 31, hi = lane >> 5;
  oa = (f32x16){};
  ob = (f32x16){};
  float ma = -INFINITY, mb = -INFINITY;
#pragma unroll 1
  for (int t = 0; t < 64; ++t) {
    const char* const p = kv + (t >> 1) * KVP_B;
    const bf16x8 k1 = *(const bf16x8*)(p + (t & 1) * 1024 + lane * 16);
    const bf16x4 k2 = *(const bf16x4*)(p + 2048 + l31 * 16 + (t & 1) * 8);
    const char* const vp = p + 2560 + (t & 1) * 1408 + hi * 352 + (l31 > 21 ? 21 : l31) * 16;
    const bf16x8 v1 = *(const bf16x8*)vp, v2 = *(const bf16x8*)(vp + 704);
    const f32x32 s2 = MFMA2B(k2, q2, ((f32x32){}));
    f32x16 sa = MFMA32(k1, q1a, lo16(s2)), sb = MFMA32(k1, q1b, hi16(s2));
    const float na = fmaxf(ma, max16(sa)), nb = fmaxf(mb, max16(sb));
    const float fa = __builtin_amdgcn_exp2f(ma - na), fb = __builtin_amdgcn_exp2f(mb - nb);
    ma = na;
    mb = nb;
#pragma unroll
    for (int r = 0; r < 16; ++r) {
      sa[r] = __builtin_amdgcn_exp2f(sa[r] - na); oa[r] *= fa;
      sb[r] = __builtin_amdgcn_exp2f(sb[r] - nb); ob[r] *= fb;
    }
    oa = MFMA32(v1, pack8(sa, 0), oa);
    oa = MFMA32(v2, pack8(sa, 8), oa);
    ob = MFMA32(v1, pack8(sb, 0), ob);
    ob = MFMA32(v2, pack8(sb, 8), ob);
  }
}
__device__ __forceinline__ float row_sum(const f32x16& o) {
  auto r = __builtin_amdgcn_permlane32_swap(__float_as_uint(o[8]), __float_as_uint(o[8]), false, false);
  return __uint_as_float(r[1]);
}
__device__ __forceinline__ bool unsafe_rows(const f32x16& o) {
  float chk = 0.f;
#pragma unroll
  for (int i = 0; i < 12; ++i) chk += fabsf(o[i]);
  return !(chk < 0x1p100f);
}
__device__ __forceinline__ int blk64(int rt, int cb) { return (rt >> 2) | ((rt & 3) << 3) | (cb << 5); }
__device__ __forceinline__ int blk128(int rt, int cb) { return ((rt >> 3) << 2) | (cb >> 2) | ((rt & 7) << 3) | ((cb & 3) << 6); }
__device__ __forceinline__ unsigned unit_off(int U) { return (U >> 3) * 1024 + ((U >> 1) & 3) * 256 + (U & 1) * 8; }

__device__ __forceinline__ void role_a(lchar* lds, int wave, int lane, char* __restrict__ ws) {
  STAMP_DECL;
  STAMP(0);
  const auto rsw = __builtin_amdgcn_make_buffer_rsrc(ws, 0, WS_END, 0x00020000);
  const int l31 = lane & 31, hi = lane >> 5;
  {
    const int head = (blockIdx.x & 7) * 4 + (blockIdx.x >> 6), pair = ((blockIdx.x >> 3) & 7) * 4 + wave;
    const char* const qp = ws + WS_Q + (head * 32 + pair) * QP_B;
    const bf16x8 q1a = *(const bf16x8*)(qp + lane * 16), q1b = *(const bf16x8*)(qp + 1024 + lane * 16);
    const bf16x4 q2 = *(const bf16x4*)(qp + 2048 + l31 * 16 + hi * 8);
    const lchar* ka = lds + lane * 16;
    const lchar* kb = lds + 2048 + l31 * 16;
    const lchar* va = lds + 2560 + hi * 352 + (l31 > 21 ? 21 : l31) * 16;
#define KOFF(t) (((t) >> 1) * KVP_B + ((t) & 1) * 1024)
#define VOFF(t) (((t) >> 1) * KVP_B + ((t) & 1) * 1408)
    BARRIER();
    STAMP(1);
    bf16x8 k1, k2, v1, v2 = {0, 0, 0, 0, 0, 0, 0, 0};
    u32x4 pa, pb = {0, 0, 0, 0};
    f32x32 negm;
    f32x16 sa[2], sb[2], oa = {}, ob = {};
    {
      k2 = *LDSP(const bf16x8, kb);
      k1 = *LDSP(const bf16x8, ka);
      v1 = *LDSP(const bf16x8, va);
      const f32x32 s2 = MFMA2B(lo4(k2), q2, ((f32x32){}));
      sa[0] = MFMA32(k1, q1a, lo16(s2));
      sb[0] = MFMA32(k1, q1b, hi16(s2));
      k1 = *LDSP(const bf16x8, ka + KOFF(1));
      const float ma = -max16(sa[0]), mb = -max16(sb[0]);
#pragma unroll
      for (int r = 0; r < 16; ++r) { negm[r] = ma; negm[16 + r] = mb; sa[0][r] += ma; sb[0][r] += mb; }
    }
#define EX(S, r) S[r] = __builtin_amdgcn_exp2f(S[r])
#define G1(S) do { EX(S, 0); EX(S, 1); EX(S, 2); EX(S, 3); EX(S, 4); PIN(S); SBAR(); } while (0)
#define G2(S, P) do { EX(S, 5); SBAR(); P[0] = pk_bf16(S[0], S[1]); SBAR(); EX(S, 6); SBAR(); P[1] = pk_bf16(S[2], S[3]); SBAR(); \
                      EX(S, 7); SBAR(); P[2] = pk_bf16(S[4], S[5]); SBAR(); P[3] = pk_bf16(S[6], S[7]); PIN(P); SBAR(); } while (0)
#define G3(S) do { EX(S, 8); EX(S, 9); EX(S, 10); EX(S, 11); EX(S, 12); PIN(S); SBAR(); } while (0)
#define G4(S, P) do { EX(S, 13); SBAR(); P[0] = pk_bf16(S[8], S[9]); SBAR(); EX(S, 14); SBAR(); P[1] = pk_bf16(S[10], S[11]); SBAR(); \
                      EX(S, 15); SBAR(); P[2] = pk_bf16(S[12], S[13]); SBAR(); P[3] = pk_bf16(S[14], S[15]); PIN(P); SBAR(); } while (0)
    int slot = 0;
#pragma unroll 1
    for (int c = 0; c < 8; ++c) {
      const int adv = (slot == 2) ? -2 * CHUNK_B : CHUNK_B;
      slot = (slot == 2) ? 0 : slot + 1;
#pragma unroll
      for (int j = 0; j < 8; ++j) {
        const int cur = j & 1, nxt = cur ^ 1;
        if (j == 6) {
          if (c < 7) asm volatile("s_barrier" ::: "memory");
          ka += adv;
          kb += adv;
        }
        SBAR();
        G1(sa[cur]);
        { const f32x32 t = MFMA2B(cur ? lo4(k2) : hi4(k2), q2, negm); sa[nxt] = lo16(t); sb[nxt] = hi16(t); }
        SBAR();
        if (!cur) { k2 = *LDSP(const bf16x8, kb + (((j + 2) & 7) >> 1) * KVP_B); SBAR(); }
        G2(sa[cur], pa);
        G3(sa[cur]);
        ob = MFMA32(v2, BC8(pb), ob); SBAR();
        v2 = *LDSP(const bf16x8, va + VOFF(j) + 704); SBAR();
        G4(sa[cur], pb);
        oa = MFMA32(v1, BC8(pa), oa); SBAR();
        G1(sb[cur]);
        sa[nxt] = MFMA32(k1, q1a, sa[nxt]); SBAR();
        G2(sb[cur], pa);
        sb[nxt] = MFMA32(k1, q1b, sb[nxt]); SBAR();
        k1 = *LDSP(const bf16x8, ka + KOFF((j + 2) & 7)); SBAR();
        G3(sb[cur]);
        oa = MFMA32(v2, BC8(pb), oa); SBAR();
        G4(sb[cur], pb);
        ob = MFMA32(v1, BC8(pa), ob); SBAR();
        if (j == 7) va += adv;
        v1 = *LDSP(const bf16x8, va + VOFF((j + 1) & 7)); SBAR();
      }
    }
    ob = MFMA32(v2, BC8(pb), ob);
#undef EX
#undef G1
#undef G2
#undef G3
#undef G4
#undef KOFF
#undef VOFF
    STAMP(2);
    if (__builtin_expect(__any(unsafe_rows(oa) || unsafe_rows(ob)), 0)) attn_slow(ws + WS_KV + head * 32 * KVP_B, q1a, q1b, q2, lane, oa, ob);
    const float ra = __builtin_amdgcn_rcpf(row_sum(oa)), rb = __builtin_amdgcn_rcpf(row_sum(ob));
    u32x2 wa[3], wb[3];
#pragma unroll
    for (int g = 0; g < 3; ++g) {
      wa[g] = (u32x2){pk_bf16(oa[4 * g] * ra, oa[4 * g + 1] * ra), pk_bf16(oa[4 * g + 2] * ra, oa[4 * g + 3] * ra)};
      wb[g] = (u32x2){pk_bf16(ob[4 * g] * rb, ob[4 * g + 1] * rb), pk_bf16(ob[4 * g + 2] * rb, ob[4 * g + 3] * rb)};
    }
    const int ob_ = WS_O + (head * 32 + pair) * QP_B;
    st128(widen32(wa[0], wb[0]), rsw, hi * 1024 + l31 * 16, ob_, SC1);
    st128(widen32(wa[1], wb[1]), rsw, hi * 1024 + l31 * 16 + 512, ob_, SC1);
    if (!hi) st128((u32x4){wa[2][0], wa[2][1], wb[2][0], wb[2][1]}, rsw, 2048 + l31 * 16, ob_, SC1);
    BARRIER();
    DRAIN();
    if (lane < 8) signal_add(counter(ws, blk64(pair, lane)));
    STAMP(3);
  }
  BARRIER();
  {
    const int rt = ((blockIdx.x & 7) >> 2) * 8 + ((blockIdx.x >> 3) & 7), cb = (blockIdx.x & 3) * 4 + (blockIdx.x >> 6);
    const int wr = wave >> 1, wc = wave & 1, G = lane >> 4;
    f32x4 acc[7][4] = {};
    bf16x8 af[2][4], bf[7];
    const lchar* const ab = lds + L_A4 + wr * 8 * 1024 + lane * 16;
    const lchar* const bb = lds + L_B4 + wc * 7 * 1024 + lane * 16;
#pragma unroll
    for (int n = 0; n < 7; ++n) bf[n] = *LDSP(const bf16x8, bb + n * 1024);
    STAMP(4);
    BARRIER();
    STAMP(5);
#pragma unroll
    for (int m = 0; m < 4; ++m) af[0][m] = *LDSP(const bf16x8, ab + 2 * m * 1024);
    u32x2 sp[4][2];
#pragma unroll
    for (int m = 0; m < 4; ++m)
#pragma unroll
      for (int i = 0; i < 2; ++i)
        sp[m][i] = __builtin_amdgcn_raw_buffer_load_b64(rsw, (2 * G + i) * 16384 + (m * 16 + (lane & 15)) * 8, WS_ST + (rt * 128 + wr * 64) * 8, SC1);
    f32x4 cs[7];
#pragma unroll
    for (int n = 0; n < 7; ++n) cs[n] = *LDSP(const f32x4, lds + L_CS + ((wc * 7 + n) * 16 + G * 4) * 4);
#define K4_MM(buf, n, m) acc[n][m] = MFMA16(bf[n], af[buf][m], acc[n][m])
    int sa_ = 0, sb_ = 0;
#pragma unroll 1
    for (int s = 0; s < 10; ++s) {
#pragma unroll
      for (int n = 0; n < 7; ++n) {
        SBAR(); K4_MM(0, n, 0); K4_MM(0, n, 1); SBAR();
        if (n < 4) af[1][n] = *LDSP(const bf16x8, ab + sa_ + (2 * n + 1) * 1024);
        SBAR(); K4_MM(0, n, 2); K4_MM(0, n, 3); SBAR();
        bf[n] = *LDSP(const bf16x8, bb + sb_ + (14 + n) * 1024);
      }
      sa_ = (sa_ == 2 * A4_B) ? 0 : sa_ + A4_B;
      sb_ = (sb_ == 2 * B4_B) ? 0 : sb_ + B4_B;
#pragma unroll
      for (int n = 0; n < 7; ++n) {
        SBAR(); K4_MM(1, n, 0); K4_MM(1, n, 1); SBAR();
        if (n >= 1 && n < 5) af[0][n - 1] = *LDSP(const bf16x8, ab + sa_ + 2 * (n - 1) * 1024);
        SBAR(); K4_MM(1, n, 2); K4_MM(1, n, 3); SBAR();
        if (n == 0) BARRIER();
        bf[n] = *LDSP(const bf16x8, bb + sb_ + n * 1024);
      }
    }
#undef K4_MM
    STAMP(6);
    float mu[4], rs[4];
#pragma unroll
    for (int m = 0; m < 4; ++m) {
      const float s = swap32_add(swap16_add(__uint_as_float(sp[m][0][0]) + __uint_as_float(sp[m][1][0])));
      const float q = swap32_add(swap16_add(__uint_as_float(sp[m][0][1]) + __uint_as_float(sp[m][1][1])));
      mu[m] = s * (1.0f / DM);
      rs[m] = __builtin_amdgcn_rsqf(fmaxf(q * (1.0f / DM) - mu[m] * mu[m], 0.f) + LN_EPS);
    }
    const int f0 = (cb * 2 + wc) * 56;
    constexpr float C0 = -2.0f * 0.7978845608028654f * 1.4426950408889634f, C1 = C0 * 0.044715f;
    auto geglu = [&](const f32x4& g, const f32x4& u, const f32x4& cg, const f32x4& cu, float m_, float r_) -> u32x2 {
      const float k0 = C0 * r_, k1 = C1 * r_ * r_ * r_, ir = __builtin_amdgcn_rcpf(r_ * r_);
      const f32x4 gg = g - m_ * cg, uu = u - m_ * cu;
      const f32x4 z = gg * (k0 + k1 * (gg * gg));
      f32x4 e, r;
#pragma unroll
      for (int j = 0; j < 4; ++j) e[j] = __builtin_amdgcn_exp2f(z[j]);
      e = ir + e * ir;
#pragma unroll
      for (int j = 0; j < 4; ++j) r[j] = __builtin_amdgcn_rcpf(e[j]);
      const f32x4 o = gg * uu * r;
      return (u32x2){pk_bf16(o[0], o[1]), pk_bf16(o[2], o[3])};
    };
    const int abase = WS_A + (rt * 8 + wr * 4) * 54 * 1024;
    if (f0 < FF) {
#pragma unroll
      for (int n = 0; n < 3; ++n) {
        const unsigned off = (G & 1) * 54 * 1024 + unit_off((f0 >> 2) + n * 4 + (G & 2)) + (lane & 15) * 16;
#pragma unroll
        for (int mp = 0; mp < 2; ++mp) {
          const u32x2 r0 = geglu(acc[n][2 * mp], acc[n + 3][2 * mp], cs[n], cs[n + 3], mu[2 * mp], rs[2 * mp]);
          const u32x2 r1 = geglu(acc[n][2 * mp + 1], acc[n + 3][2 * mp + 1], cs[n], cs[n + 3], mu[2 * mp + 1], rs[2 * mp + 1]);
          st128(widen16(r0, r1), rsw, off, abase + mp * 2 * 54 * 1024, SC1);
        }
      }
    }
    if (f0 + 48 < FF) {
      f32x4 cg, cu;
#pragma unroll
      for (int j = 0; j < 4; ++j) {
        auto r = __builtin_amdgcn_permlane32_swap(__float_as_uint(cs[6][j]), __float_as_uint(cs[6][j]), false, false);
        cg[j] = __uint_as_float(r[0]);
        cu[j] = __uint_as_float(r[1]);
      }
      u32x2 r2[2];
#pragma unroll
      for (int mp = 0; mp < 2; ++mp) {
        f32x4 g, u;
#pragma unroll
        for (int j = 0; j < 4; ++j) {
          auto r = __builtin_amdgcn_permlane32_swap(__float_as_uint(acc[6][2 * mp][j]), __float_as_uint(acc[6][2 * mp + 1][j]), false, false);
          g[j] = __uint_as_float(r[0]);
          u[j] = __uint_as_float(r[1]);
        }
        r2[mp] = geglu(g, u, cg, cu, hi ? mu[2 * mp + 1] : mu[2 * mp], hi ? rs[2 * mp + 1] : rs[2 * mp]);
      }
      st128(widen16(r2[0], r2[1]), rsw, (2 * (G & 1) + hi) * 54 * 1024 + unit_off((f0 >> 2) + 12) + (lane & 15) * 16, abase, SC1);
    }
    STAMP(7);
    DRAIN();
    if (lane < 8) signal_add(counter(ws, 512 + blk64(rt * 2 + wr, lane)));
    STAMP(8);
  }
  BARRIER();
  {
    const unsigned lbase = (unsigned)(uintptr_t)lds;
    const char* const wb = ws + WS_WD + ((blockIdx.x >> 5) * 54 + 32) * 5 * 1024;
    BARRIER();
    dma_lin<10>(wb + wave * 10240, lane * 16, lbase + wave * 10240);
    BARRIER();
    dma_lin<10>(wb + 40960 + wave * 10240, lane * 16, lbase + 40960 + wave * 10240);
    BARRIER();
    const int o = 81920 + min(wave * 8, 22) * 1024;
    dma_lin<8>(wb + o, lane * 16, lbase + o);
    WAITV_BARRIER(18);
    WAITV_BARRIER(8);
    WAITV_BARRIER(0);
  }
  STAMP_OUT(ws, 1);
}

__device__ __forceinline__ void role_b(lchar* lds, int lw, int lane, const char* __restrict__ x, const char* __restrict__ wo,
                                       const char* __restrict__ wg, const char* __restrict__ wu, const char* __restrict__ wd,
                                       char* __restrict__ ws, char* __restrict__ out) {
  STAMP_DECL;
  STAMP(0);
  const auto rsw = __builtin_amdgcn_make_buffer_rsrc(ws, 0, WS_END, 0x00020000);
  const unsigned lbase = (unsigned)(uintptr_t)lds;
  const int rt = (blockIdx.x & 7) * 4 + ((blockIdx.x >> 3) & 3), cb = blockIdx.x >> 5;
  const int rt4 = ((blockIdx.x & 7) >> 2) * 8 + ((blockIdx.x >> 3) & 7), cb4 = (blockIdx.x & 3) * 4 + (blockIdx.x >> 6);
  const int rh = lw & 1, kp = lw >> 1, tt = rh * 2 + kp, G = lane >> 4;
  const int tok = (rt * 4 + tt) * 16 + (lane & 15);
  u32x2 res[5];
  float csum = 0.f;
  {
    const char* const kv = ws + WS_KV + ((blockIdx.x & 7) * 4 + (blockIdx.x >> 6)) * 32 * KVP_B + lw * 7168;
    int ch = 0, slot = 0;
    auto issue = [&]() {
      if (lw < 3) dma_lin<7>(kv + ch * CHUNK_B, lane * 16, lbase + slot + lw * 7168);
      ++ch;
      slot = (slot == 2 * CHUNK_B) ? 0 : slot + CHUNK_B;
    };
    issue();
    issue();
    WAITV_BARRIER(7);
    issue();
    const int unit = blockIdx.x * 4 + lw;
    const bool gu = unit < 620;
    int uks, uc;
    {
      const char *s0, *s1;
      unsigned voff;
      int pitch, r0, r1, r2;
      if (gu) {
        uks = unit / 31; uc = unit - uks * 31; pitch = FF * 2;
        s0 = wg + uc * 112; s1 = wu + uc * 112;
        voff = (lane >> 3) * pitch + min(lane & 7, uc == 30 ? 5 : 6) * 16;
        r0 = uks * 32; r1 = 8; r2 = 16;
      } else {
        const int u = unit < 890 ? unit - 620 : unit < 990 ? unit - 890 : 0;
        uks = u / 5; uc = u - uks * 5; pitch = DM * 2;
        s0 = (unit < 890 ? wd : wo) + uc * 256; s1 = s0 + 128;
        int row = lane >> 3;
        if (unit < 890) { r0 = uks * 32; r1 = 8; r2 = 16; }
        else if (uks < 16) { r0 = uks * 40; r1 = 8; r2 = 20; }
        else { r0 = (uks - 16) * 160 + 16; r1 = 40; r2 = 80; row = (row >> 2) * 20 + (row & 3); }
        voff = row * pitch + (lane & 7) * 16;
      }
#pragma unroll
      for (int i = 0; i < 4; ++i) {
        dma16(s0 + (size_t)(r0 + (i & 1) * r1 + (i >> 1) * r2) * pitch, voff, lbase + L_SCR + lw * 8192 + i * 1024);
        dma16(s1 + (size_t)(r0 + (i & 1) * r1 + (i >> 1) * r2) * pitch, voff, lbase + L_SCR + lw * 8192 + 4096 + i * 1024);
      }
    }
    WAITV_BARRIER(15);
    WAITV_BARRIER(8);
    issue();
    WAITV_BARRIER(0);
    issue();
    {
      const int p = lane & 3;
      const lchar* const sp = lds + L_SCR + lw * 8192 + (8 * G + ((lane & 15) >> 2)) * 128 + p * 8;
      if (gu) {
        const bf16x8 ones = {0x3f80, 0x3f80, 0x3f80, 0x3f80, 0x3f80, 0x3f80, 0x3f80, 0x3f80};
        const int dst = WS_WGU + (((uc >> 1) * 20 + uks) * 14 + (uc & 1) * 7) * 1024;
        float c0 = 0.f, c1 = 0.f;
#pragma unroll
        for (int t = 0; t < 7; ++t) {
          const lchar* const q = t < 3 ? sp + t * 32 : t < 6 ? sp + 4096 + (t - 3) * 32 : sp + 96 + (p >> 1) * (4096 - 16);
          const bf16x8 fr = cat8(tr_read(q), tr_read(q + 512));
          st128(__builtin_bit_cast(u32x4, fr), rsw, lane * 16, dst + t * 1024, SC1);
          const f32x4 cs = MFMA16(ones, fr, ((f32x4){0.f, 0.f, 0.f, 0.f}));
          if (t < 4) c0 = (G == t) ? cs[0] : c0;
          else c1 = (G == t - 4) ? cs[0] : c1;
        }
        const int cp = WS_CSP + (uks * 3584 + uc * 112) * 4;
        __builtin_amdgcn_raw_buffer_store_b32(__float_as_uint(c0), rsw, lane * 4, cp, SC1);
        if (lane < 48) __builtin_amdgcn_raw_buffer_store_b32(__float_as_uint(c1), rsw, lane * 4 + 256, cp, SC1);
      } else if (unit < 990) {
        const int dst = unit < 890 ? WS_WD : WS_WO, nks = unit < 890 ? 54 : 20;
#pragma unroll
        for (int t = 0; t < 8; ++t) {
          const lchar* const q = sp + (t >> 2) * 4096 + (t & 3) * 32;
          const bf16x8 fr = cat8(tr_read(q), tr_read(q + 512));
          const int t16 = uc * 8 + t, c5 = (t16 * 205) >> 10, tile = t16 - c5 * 5;
          st128(__builtin_bit_cast(u32x4, fr), rsw, lane * 16, dst + ((c5 * nks + uks) * 5 + tile) * 1024, SC1);
        }
      }
    }
    WAITV_BARRIER(0);
    if (lw == 3 && lane == 0) signal_add(counter(ws, 768 + (blockIdx.x & 7)));
    issue();
#pragma unroll
    for (int n = 0; n < 5; ++n) res[n] = *(const u32x2*)(x + (size_t)tok * 1280 + (cb * 20 + n * 4 + G) * 8);
    WAITV_BARRIER(0);
    issue();
    if (lw == 3) wait_count(counter(ws, 768 + (lane & 7)), 32);
    WAITV_BARRIER(0);
    issue();
    const char* const wop = ws + WS_WO + cb * 100 * 1024;
    dma_lin<12>(wop + (4 + lw * 12) * 1024, lane * 16, lbase + L_WO + (4 + lw * 12) * 1024);
    WAITV_BARRIER(12);
    dma_lin<12>(wop + (52 + lw * 12) * 1024, lane * 16, lbase + L_WO + (52 + lw * 12) * 1024);
    dma_run<1>(wop + lw * 1024, lane * 16, lbase + L_WO + lw * 1024);
#pragma unroll
    for (int i = 0; i < 20; ++i)
      csum += __uint_as_float(__builtin_amdgcn_raw_buffer_load_b32(rsw, min(lane, 55) * 4, WS_CSP + (i * 3584 + cb4 * 224 + lw * 56) * 4, SC1));
    {
      const char* const p = ws + WS_WGU + (blockIdx.x & 3) * 4 * 280 * 1024 + (blockIdx.x >> 3) * 35840 + lw * 8960;
      touch64(p, lane * 128, lbase + 2 * CHUNK_B + 8192);
      touch64(p, min(8192 + lane * 128, 8832), lbase + 2 * CHUNK_B + 8192);
    }
    STAMP(1);
    WAITV_BARRIER(0);
    STAMP(2);
  }
  const char* const wgu = ws + WS_WGU + cb4 * 280 * 1024;
  {
    dma_lin<14>(wgu + lw * 14 * 1024, lane * 16, lbase + L_B4 + lw * 14 * 1024);
    const lchar* const wb = lds + L_WO + lane * 16;
    bf16x8 wf[3][5];
#pragma unroll
    for (int ks = 0; ks < 2; ++ks)
#pragma unroll
      for (int n = 0; n < 5; ++n) wf[ks][n] = *LDSP(const bf16x8, wb + (ks * 5 + n) * 1024);
    DRAIN();
    wait_count(counter(ws, blockIdx.x), 32);
    STAMP(3);
    const int key = (tt & 1) * 16 + (lane & 15);
    u32x4 a[20];
#pragma unroll
    for (int ks = 0; ks < 16; ++ks)
      a[ks] = __builtin_amdgcn_raw_buffer_load_b128(rsw, (G >> 1) * 32 * QP_B + (tt >> 1) * 1024 + (G & 1) * 512 + key * 16, WS_O + (2 * ks * 32 + rt) * QP_B, SC1);
#pragma unroll
    for (int ks = 16; ks < 20; ++ks) {
      const unsigned vo = 2 * G * 32 * QP_B + 2048 + key * 16 + (tt >> 1) * 8;
      const u32x2 lo = __builtin_amdgcn_raw_buffer_load_b64(rsw, vo, WS_O + (8 * (ks - 16) * 32 + rt) * QP_B, SC1);
      const u32x2 hi2 = __builtin_amdgcn_raw_buffer_load_b64(rsw, vo, WS_O + ((8 * (ks - 16) + 1) * 32 + rt) * QP_B, SC1);
      a[ks] = (u32x4){lo[0], lo[1], hi2[0], hi2[1]};
    }
    f32x4 acc[5] = {};
#pragma unroll
    for (int ks = 0; ks < 20; ++ks)
#pragma unroll
      for (int n = 0; n < 5; ++n) {
        SBAR();
        if (ks + 2 < 20) wf[(ks + 2) % 3][n] = *LDSP(const bf16x8, wb + ((ks + 2) * 5 + n) * 1024);
        SBAR();
        acc[n] = MFMA16(wf[ks % 3][n], BC8(a[ks]), acc[n]);
      }
    SBAR();
    STAMP(4);
    float s = 0.f, q = 0.f;
#pragma unroll
    for (int n = 0; n < 5; ++n) {
      const unsigned w0 = pk_bf16(acc[n][0] + bf_lo(res[n][0]), acc[n][1] + bf_hi(res[n][0]));
      const unsigned w1 = pk_bf16(acc[n][2] + bf_lo(res[n][1]), acc[n][3] + bf_hi(res[n][1]));
      res[n] = (u32x2){w0, w1};
      const float h0 = bf_lo(w0), h1 = bf_hi(w0), h2 = bf_lo(w1), h3 = bf_hi(w1);
      s += (h0 + h1) + (h2 + h3);
      q += (h0 * h0 + h1 * h1) + (h2 * h2 + h3 * h3);
    }
    const int hb = WS_H + (rt * 4 + tt) * 20480;
#pragma unroll
    for (int np = 0; np < 2; ++np)
      st128(widen16(res[2 * np], res[2 * np + 1]), rsw, unit_off(cb * 20 + (2 * np + (G & 1)) * 4 + (G & 2)) + (lane & 15) * 16, hb, SC1);
    __builtin_amdgcn_raw_buffer_store_b64(res[4], rsw, unit_off(cb * 20 + 16 + G) + (lane & 15) * 16, hb, SC1);
    s = swap32_add(swap16_add(s));
    q = swap32_add(swap16_add(q));
    if (lane < 16) __builtin_amdgcn_raw_buffer_store_b64((u32x2){__float_as_uint(s), __float_as_uint(q)}, rsw, tok * 8, WS_ST + cb * 16384, SC1);
    BARRIER();
    DRAIN();
    if (lane < 16) signal_add(counter(ws, 256 + blk128(rt >> 1, lane)));
    STAMP(5);
  }
  {
    if (lane < 56) *LDSP(float, lds + L_CS + (lw * 56 + lane) * 4) = csum;
    dma_lin<7>(wgu + 2 * B4_B + lw * 7168, lane * 16, lbase + L_B4 + 2 * B4_B + lw * 7168);
    wait_count(counter(ws, 256 + blockIdx.x), 64);
    STAMP(6);
    const char* const ha = ws + WS_H + (rt4 * 8 + lw * 2) * 20480;
    int sa_ = 0, sb_ = 0;
    auto issue_a = [&](int s) {
      dma_run<2>(ha + s * 2048, lane * 16, lbase + L_A4 + sa_ + lw * 4096);
      dma_run<2>(ha + 20480 + s * 2048, lane * 16, lbase + L_A4 + sa_ + lw * 4096 + 2048);
      sa_ = (sa_ == 2 * A4_B) ? 0 : sa_ + A4_B;
    };
    issue_a(0);
    issue_a(1);
    WAITV_BARRIER(4);
    issue_a(2);
    WAITV_BARRIER(4);
#pragma unroll 1
    for (int s = 3; s < 10; ++s) {
      issue_a(s);
      dma_lin<7>(wgu + s * B4_B + lw * 7168, lane * 16, lbase + L_B4 + sb_ + lw * 7168);
      sb_ = (sb_ == 2 * B4_B) ? 0 : sb_ + B4_B;
      WAITV_BARRIER(11);
    }
    WAITV_BARRIER(0);
    BARRIER();
    STAMP(7);
  }
  {
    dma_lin<40>(ws + WS_WD + cb * 270 * 1024 + lw * 40960, lane * 16, lbase + lw * 40960);
    STAMP(8);
    WAITV_BARRIER(0);
    const lchar* const wb = lds + kp * 5120 + lane * 16;
    bf16x8 wf[2][5];
#pragma unroll
    for (int n = 0; n < 5; ++n) wf[0][n] = *LDSP(const bf16x8, wb + n * 1024);
    STAMP(9);
    wait_count(counter(ws, 512 + blockIdx.x), 32);
    STAMP(10);
    constexpr int WIN = 10;
    u32x4 a[WIN][2];
    const int a0 = WS_A + ((rt * 4 + tt) * 54 + kp) * 1024, a1 = WS_A + ((rt * 4 + (tt ^ 1)) * 54 + kp) * 1024;
#pragma unroll
    for (int i = 0; i < WIN; ++i) {
      a[i][0] = __builtin_amdgcn_raw_buffer_load_b128(rsw, lane * 16, a0 + i * 2048, SC1);
      a[i][1] = __builtin_amdgcn_raw_buffer_load_b128(rsw, lane * 16, a1 + i * 2048, SC1);
    }
    f32x4 acc[2][5] = {};
#pragma unroll
    for (int i = 0; i < 27; ++i) {
      if ((i & 3) == 3) BARRIER();
#pragma unroll
      for (int n = 0; n < 5; ++n) {
        SBAR();
        if (i + 1 < 27) wf[(i + 1) & 1][n] = *LDSP(const bf16x8, wb + (((i + 1) & 15) * 10 + n) * 1024);
        SBAR();
        acc[0][n] = MFMA16(wf[i & 1][n], BC8(a[i % WIN][0]), acc[0][n]);
        acc[1][n] = MFMA16(wf[i & 1][n], BC8(a[i % WIN][1]), acc[1][n]);
      }
      SBAR();
      if (i + WIN < 27) {
        a[i % WIN][0] = __builtin_amdgcn_raw_buffer_load_b128(rsw, lane * 16, a0 + (i + WIN) * 2048, SC1);
        a[i % WIN][1] = __builtin_amdgcn_raw_buffer_load_b128(rsw, lane * 16, a1 + (i + WIN) * 2048, SC1);
      }
    }
    SBAR();
    STAMP(11);
#pragma unroll
    for (int n = 0; n < 5; ++n) *LDSP(f32x4, lds + L_XCH + (lw * 5 + n) * 1024 + lane * 16) = acc[1][n];
    BARRIER();
    u32x2 o[5];
#pragma unroll
    for (int n = 0; n < 5; ++n) {
      const f32x4 v = acc[0][n] + *LDSP(const f32x4, lds + L_XCH + ((lw ^ 2) * 5 + n) * 1024 + lane * 16);
      o[n] = (u32x2){pk_bf16(v[0] + bf_lo(res[n][0]), v[1] + bf_hi(res[n][0])), pk_bf16(v[2] + bf_lo(res[n][1]), v[3] + bf_hi(res[n][1]))};
    }
    char* const op = out + (size_t)tok * 1280 + cb * 160;
#pragma unroll
    for (int np = 0; np < 2; ++np) *(u32x4*)(op + ((2 * np + (G & 1)) * 4 + (G & 2)) * 8) = widen16(o[2 * np], o[2 * np + 1]);
    *(u32x2*)(op + (16 + G) * 8) = o[4];
    STAMP(12);
  }
  STAMP_OUT(ws, 1);
}

__global__ __launch_bounds__(512, 2) void k2_rest(const char* __restrict__ x, const char* __restrict__ wo, const char* __restrict__ wg,
                                                  const char* __restrict__ wu, const char* __restrict__ wd, char* __restrict__ ws,
                                                  char* __restrict__ out) {
  __shared__ __attribute__((aligned(1024))) char smem[163840];
  const int lane = threadIdx.x & 63, wave = __builtin_amdgcn_readfirstlane(threadIdx.x >> 6);
  if (wave >= 4) role_b((lchar*)smem, wave - 4, lane, x, wo, wg, wu, wd, ws, out);
  else role_a((lchar*)smem, wave, lane, ws);
}
__global__ void k3_end() {}

extern "C" void kernel_launch(void* const* d_in, const int* in_sizes, int n_in, void* d_out, int out_size, void* d_ws, size_t ws_size,
                              hipStream_t stream) {
  const char* x = (const char*)d_in[0];
  char* ws = (char*)d_ws;
  k1_qkv<<<256, 768, 0, stream>>>(x, (const char*)d_in[1], ws);
  k2_rest<<<256, 512, 0, stream>>>(x, (const char*)d_in[2], (const char*)d_in[4], (const char*)d_in[5], (const char*)d_in[6], ws, (char*)d_out);
}
```

```cpp
#include <hip/hip_runtime.h>
#include <hip/hip_bf16.h>
#include <stdint.h>

typedef __attribute__((ext_vector_type(8))) short bf16x8;
typedef __attribute__((ext_vector_type(4))) short bf16x4;
typedef __attribute__((ext_vector_type(32))) float f32x32;
typedef __attribute__((ext_vector_type(16))) float f32x16;
typedef __attribute__((ext_vector_type(4))) float f32x4;
typedef __attribute__((ext_vector_type(2))) float f32x2;
typedef __attribute__((ext_vector_type(4))) unsigned u32x4;
typedef __attribute__((ext_vector_type(2))) unsigned u32x2;
typedef unsigned long long u64;
typedef __attribute__((address_space(3))) char lchar;
#define LDSP(T, p) ((__attribute__((address_space(3))) T*)(p))

#define TLBWARM 1
#define K1UNROLL 0
#define CODEPF_BLOCKS 16
#define SWAPROLE 0
#define ROT2 0
#define ROT4 0
#define ROT5 3
#define P5WAIT 6
#define PF_INFLIGHT 4
constexpr int DM = 640, FF = 1728;
constexpr float LN_EPS = 1e-5f;
constexpr float QSCALE = 0.22360679774997896f * 1.4426950408889634f;

constexpr int QP_B = 2560, KVP_B = 5376;
constexpr int WS_Q = 0;
constexpr int WS_KV = WS_Q + 32 * 32 * QP_B;
constexpr int WS_O = WS_KV + 32 * 32 * KVP_B;
constexpr int WS_H = WS_O + 32 * 32 * QP_B;
constexpr int WS_A = WS_H + 128 * 20 * 1024;
constexpr int WS_WO = WS_A + 128 * 54 * 1024;
constexpr int WS_WGU = WS_WO + 8 * 20 * 5 * 1024;
constexpr int WS_WD = WS_WGU + 16 * 20 * 14 * 1024;
constexpr int WS_CSP = WS_WD + 8 * 54 * 5 * 1024;
constexpr int WS_ST = WS_CSP + 20 * 3584 * 4;
constexpr int WS_CNT = WS_ST + 8 * 2048 * 8;
constexpr int WS_RS = WS_CNT + 256 * 24 * 128;
constexpr int WS_FS = WS_RS + 2048 * 8;
constexpr int WS_SALT = WS_FS + 256 * 8;
constexpr int WS_END = WS_SALT + 128;

#define MFMA32(a, b, c) __builtin_amdgcn_mfma_f32_32x32x16_bf16(a, b, c, 0, 0, 0)
#define MFMA16(a, b, c) __builtin_amdgcn_mfma_f32_16x16x32_bf16(a, b, c, 0, 0, 0)
#define MFMA2B(a, b, c) __builtin_amdgcn_mfma_f32_32x32x4bf16_1k(a, b, c, 0, 0, 0)
#define MFMA16_ACC(acc, a, b) asm volatile("v_mfma_f32_16x16x32_bf16 %0, %1, %2, %0" : "+v"(acc) : "v"(a), "v"(b))
#define MFMA_DONE() asm volatile("s_nop 7\n\ts_nop 1" ::: "memory")
#define PIN(x) asm volatile("" : "+v"(x))
#define PINS(x) asm volatile("" : "+s"(x))
#define SBAR() __builtin_amdgcn_sched_barrier(0)
#define BARRIER() asm volatile("s_waitcnt lgkmcnt(0)\n\ts_barrier" ::: "memory")
#define WAITV_BARRIER(N) asm volatile("s_waitcnt vmcnt(%0) lgkmcnt(0)\n\ts_barrier" ::"i"(N) : "memory")
#define DRAIN() asm volatile("s_waitcnt vmcnt(0)" ::: "memory")
#define BC8(x) __builtin_bit_cast(bf16x8, x)
constexpr int SC1 = 16;

#define STAMP(i)
#define STAMP_DECL
#define STAMP_ARG
#define STAMP_PASS
#define STAMP_OUT(ws, kid)

__device__ __forceinline__ void dma16(const void* sbase, unsigned voff, unsigned lds) {
  asm volatile("s_mov_b32 m0, %2\n\ts_nop 0\n\tglobal_load_lds_dwordx4 %0, %1" ::"v"(voff), "s"(sbase), "s"(lds) : "memory");
}
template <int N>
__device__ __forceinline__ void dma_run(const void* sbase, unsigned voff, unsigned lds) {
#define GL(o) "\n\tglobal_load_lds_dwordx4 %0, %1 offset:" #o " sc1"
  if constexpr (N == 1) asm volatile("s_mov_b32 m0, %2\n\ts_nop 0" GL(0)::"v"(voff), "s"(sbase), "s"(lds) : "memory");
  else if constexpr (N == 2) asm volatile("s_mov_b32 m0, %2\n\ts_nop 0" GL(0) GL(1024)::"v"(voff), "s"(sbase), "s"(lds) : "memory");
  else if constexpr (N == 3) asm volatile("s_mov_b32 m0, %2\n\ts_nop 0" GL(0) GL(1024) GL(2048)::"v"(voff), "s"(sbase), "s"(lds) : "memory");
  else asm volatile("s_mov_b32 m0, %2\n\ts_nop 0" GL(0) GL(1024) GL(2048) GL(3072)::"v"(voff), "s"(sbase), "s"(lds) : "memory");
#undef GL
}
template <int N>
__device__ __forceinline__ void dma_lin(const char* sbase, unsigned voff, unsigned lds) {
#pragma unroll
  for (int i = 0; i + 4 <= N; i += 4) dma_run<4>(sbase + i * 1024, voff, lds + i * 1024);
  if constexpr (N % 4 != 0) dma_run<N % 4>(sbase + (N / 4 * 4) * 1024, voff, lds + (N / 4 * 4) * 1024);
}
__device__ __forceinline__ void touch64(const void* sbase, unsigned voff, unsigned lds) {
  asm volatile("s_mov_b32 m0, %2\n\ts_nop 0\n\tglobal_load_lds_dword %0, %1 sc1" ::"v"(voff), "s"(sbase), "s"(lds) : "memory");
}
__device__ __forceinline__ unsigned pk_bf16(float lo, float hi) {
  typedef __attribute__((ext_vector_type(2))) __bf16 bf2;
  bf2 r = __builtin_convertvector((f32x2){lo, hi}, bf2);
  return __builtin_bit_cast(unsigned, r);
}
__device__ __forceinline__ float bf_lo(unsigned w) { return __uint_as_float(w << 16); }
__device__ __forceinline__ float bf_hi(unsigned w) { return __uint_as_float(w & 0xffff0000u); }
__device__ __forceinline__ bf16x4 tr_read(const lchar* p) { return __builtin_amdgcn_ds_read_tr16_b64_v4i16(LDSP(bf16x4, p)); }
__device__ __forceinline__ bf16x8 cat8(bf16x4 a, bf16x4 b) { return __builtin_shufflevector(a, b, 0, 1, 2, 3, 4, 5, 6, 7); }
__device__ __forceinline__ float swap32_add(float v) {
  auto r = __builtin_amdgcn_permlane32_swap(__float_as_uint(v), __float_as_uint(v), false, false);
  return __uint_as_float(r[0]) + __uint_as_float(r[1]);
}
__device__ __forceinline__ float swap16_add(float v) {
  auto r = __builtin_amdgcn_permlane16_swap(__float_as_uint(v), __float_as_uint(v), false, false);
  return __uint_as_float(r[0]) + __uint_as_float(r[1]);
}
__device__ __forceinline__ u32x4 widen32(u32x2 a, u32x2 b) {
  auto r0 = __builtin_amdgcn_permlane32_swap(a[0], b[0], false, false);
  auto r1 = __builtin_amdgcn_permlane32_swap(a[1], b[1], false, false);
  return (u32x4){r0[0], r1[0], r0[1], r1[1]};
}
__device__ __forceinline__ u32x4 widen16(u32x2 a, u32x2 b) {
  auto r0 = __builtin_amdgcn_permlane16_swap(a[0], b[0], false, false);
  auto r1 = __builtin_amdgcn_permlane16_swap(a[1], b[1], false, false);
  return (u32x4){r0[0], r1[0], r0[1], r1[1]};
}
template <typename R>
__device__ __forceinline__ void st128(u32x4 v, R rsrc, unsigned voff, int soff, int aux) {
  __builtin_amdgcn_raw_buffer_store_b128(v, rsrc, voff + soff, 0, SC1);
}
constexpr int C_O = 0, C_H = 4, C_A = 8, C_KV = 12, C_Q = 20, C_W = 21;
__device__ __forceinline__ unsigned* counter(char* ws, int blk, int i) { return (unsigned*)(ws + WS_CNT) + (blk * 24 + i) * 32; }
__device__ __forceinline__ void signal_add(unsigned* c) { __hip_atomic_fetch_add(c, 1u, __ATOMIC_RELAXED, __HIP_MEMORY_SCOPE_AGENT); }
__device__ __forceinline__ u64 load_tag(const char* ws) {
  u64 t;
  asm volatile("s_load_dwordx2 %0, %1, 0x0\n\ts_waitcnt lgkmcnt(0)" : "=s"(t) : "s"(ws + WS_SALT) : "memory");
  return t + 1;
}
__device__ __forceinline__ u64 flag_load(const char* p) { return __hip_atomic_load((const u64*)p, __ATOMIC_RELAXED, __HIP_MEMORY_SCOPE_AGENT); }
__device__ __forceinline__ void flag_store(char* p, u64 v) { __hip_atomic_store((u64*)p, v, __ATOMIC_RELAXED, __HIP_MEMORY_SCOPE_AGENT); }
__device__ __forceinline__ void wait_started(const char* ws, u64 tag, int lane) {
#pragma unroll 1
  for (unsigned spins = 0; spins < (1u << 18); ++spins) {
    bool ok = true;
#pragma unroll
    for (int i = 0; i < 4; ++i) ok &= flag_load(ws + WS_FS + (i * 64 + lane) * 8) == tag;
    if (__all(ok)) break;
    __builtin_amdgcn_s_sleep(4);
  }
  asm volatile("" ::: "memory");
}
__device__ __forceinline__ void wait_count(unsigned* c, unsigned want) {
  for (unsigned spins = 0; spins < (1u << 22); ++spins) {
    if (__all(__hip_atomic_load(c, __ATOMIC_RELAXED, __HIP_MEMORY_SCOPE_AGENT) >= want)) break;
    __builtin_amdgcn_s_sleep(1);
  }
  asm volatile("" ::: "memory");
}

constexpr int L_X1 = 0, L_W1 = 3 * 16384;
constexpr int L_RS = 6 * 16384;
template <bool ISV, typename R, typename F>
__device__ __forceinline__ void qkv_tile(lchar* lds, int wave, int lane, int rt, int ct, R rsw, F&& stats STAMP_ARG) {
  const int wr = wave >> 1, wc = wave & 1, l15 = lane & 15, G = lane >> 4, hi = lane >> 5;
  const lchar* xa[2];
#pragma unroll
  for (int ks = 0; ks < 2; ++ks) { xa[ks] = lds + L_X1 + (wr * 64 + l15) * 128 + 16 * ((4 * ks + G) ^ (l15 >> 1)); PIN(xa[ks]); }
  const lchar* wa[4][2];
  {
    const int q = l15 >> 2, p = lane & 3;
#pragma unroll
    for (int n = 0; n < 4; ++n)
#pragma unroll
      for (int t = 0; t < 2; ++t) {
        wa[n][t] = lds + L_W1 + (8 * G + 4 * t + q) * 256 + 16 * ((2 * (4 * wc + (n ^ (2 * wr))) + (p >> 1)) ^ ((q << 2) | ((2 * G + t) & 3))) + 8 * (p & 1);
        PIN(wa[n][t]);
      }
  }
  const int sec = ct / 5, cbase = (ct - sec * 5) * 128 + wc * 64;
  unsigned so[4];
  u64 swp[4];
  if (!ISV) {
    const int pstride = sec == 0 ? QP_B : KVP_B, base = (sec == 0 ? WS_Q : WS_KV) + (rt * 2 + wr) * pstride;
#pragma unroll
    for (int n = 0; n < 4; ++n) {
      const int P = ((cbase + (n ^ (2 * wr)) * 16) >> 3) + hi, hp = (P * 205) >> 10, pm = P - hp * 5;
      so[n] = base + l15 * 16 + (pm == 2 ? (hp * 2 + (G & 1)) * 32 * pstride + 2048
                                         : (hp * 2 + (pm > 2)) * 32 * pstride + (G & 1) * 1024 + ((pm == 1 || pm == 4) ? 512 : 0));
      swp[n] = __ballot(pm != 2);
      PIN(so[n]); PINS(swp[n]);
    }
  } else {
#pragma unroll
    for (int n = 0; n < 4; ++n) {
      const int cc = cbase + (n ^ (2 * wr)) * 16 + l15, head = (cc * 3277) >> 16, d = cc - head * 20;
      so[n] = WS_KV + (head * 32 + rt * 2 + wr) * KVP_B + 2560 + d * 128 + 16 * (G ^ ((d >> 1) & 7));
      PIN(so[n]);
    }
  }
  f32x4 acc[4][4] = {}, cs[2] = {};
  bf16x8 ones = {0x3f80, 0x3f80, 0x3f80, 0x3f80, 0x3f80, 0x3f80, 0x3f80, 0x3f80};
#pragma unroll
  for (int n = 0; n < 4; ++n)
#pragma unroll
    for (int m = 0; m < 4; ++m) PIN(acc[n][m]);
  PIN(cs[0]); PIN(cs[1]); PIN(ones);
  bf16x8 xf[2][4], wf[4];
#define P1_MM(buf, n, m) do { SBAR(); if (ISV) MFMA16_ACC(acc[n][m], xf[buf][m], wf[n]); else MFMA16_ACC(acc[n][m], wf[n], xf[buf][m]); SBAR(); } while (0)
#define P1_CS(n) do { SBAR(); MFMA16_ACC(cs[n], ones, wf[n]); SBAR(); } while (0)
#define P1_RX(buf, sl, ks, m) xf[buf][m] = *LDSP(const bf16x8, xa[ks] + (sl) * 16384 + (m) * 2048)
#define P1_RW(sl, ks, n) wf[n] = cat8(tr_read(wa[n][0] + (sl) * 16384 + (ks) * 8192), tr_read(wa[n][1] + (sl) * 16384 + (ks) * 8192))
#define P1_H1(sl)                                                                                   \
  _Pragma("unroll") for (int n = 0; n < 4; ++n) {                                                   \
    P1_MM(0, n, 0); if (n < 2) P1_RX(1, sl, 1, 2 * n); P1_MM(0, n, 1);                              \
    if (n < 2) P1_RX(1, sl, 1, 2 * n + 1);                                                          \
    P1_MM(0, n, 2); P1_MM(0, n, 3);                                                                 \
    if (n < 2) P1_CS(n);                                                                            \
    P1_RW(sl, 1, n);                                                                                \
  }
#define P1_H2(sl)                                                                                   \
  do {                                                                                              \
    P1_MM(1, 0, 0); P1_MM(1, 0, 1); P1_MM(1, 0, 2); P1_MM(1, 0, 3); P1_CS(0);                       \
    P1_MM(1, 1, 0); P1_MM(1, 1, 1);                                                                 \
    WAITV_BARRIER(0);                                                                               \
    P1_RW(sl, 0, 0); P1_MM(1, 1, 2); P1_RX(0, sl, 0, 0); P1_MM(1, 1, 3); P1_RX(0, sl, 0, 1);        \
    P1_CS(1); P1_RW(sl, 0, 1);                                                                      \
    P1_MM(1, 2, 0); P1_RX(0, sl, 0, 2); P1_MM(1, 2, 1); P1_RX(0, sl, 0, 3); P1_MM(1, 2, 2); P1_MM(1, 2, 3); \
    P1_RW(sl, 0, 2);                                                                                \
    P1_MM(1, 3, 0); P1_MM(1, 3, 1); P1_MM(1, 3, 2); P1_MM(1, 3, 3);                                 \
    P1_RW(sl, 0, 3);                                                                                \
  } while (0)
  SBAR();
  stats();
  STAMP(12);
  BARRIER();
  STAMP(13);
#pragma unroll
  for (int i = 0; i < 4; ++i) { P1_RW(0, 0, i); P1_RX(0, 0, 0, i); }
  __builtin_amdgcn_s_waitcnt(0xC07F);
#pragma unroll 1
  for (int s = 0; s < 3; ++s) {
    P1_H1(0); P1_H2(1);
    P1_H1(1); P1_H2(2);
    P1_H1(2); P1_H2(0);
  }
  P1_H1(0);
  P1_CS(0); P1_CS(1);
#pragma unroll
  for (int m = 0; m < 4; ++m) P1_MM(1, 0, m);
  if (lane < 16) {
    *LDSP(float, lds + L_RS + 1024 + (wc * 64 + wr * 32 + lane) * 4) = cs[0][0];
    *LDSP(float, lds + L_RS + 1024 + (wc * 64 + wr * 32 + 16 + lane) * 4) = cs[1][0];
  }
#pragma unroll
  for (int m = 0; m < 4; ++m) P1_MM(1, 1, m);
  BARRIER();
  const lchar* const st = lds + L_RS;
  if (!ISV) {
    f32x2 mr[4];
    f32x4 c4[4];
#pragma unroll
    for (int i = 0; i < 4; ++i) {
      mr[i] = *LDSP(const f32x2, st + (wr * 64 + i * 16 + l15) * 8);
      c4[i] = *LDSP(const f32x4, st + 1024 + (wc * 64 + (i ^ (2 * wr)) * 16 + 4 * G) * 4);
    }
#pragma unroll
    for (int n = 2; n < 4; ++n)
#pragma unroll
      for (int m = 0; m < 4; ++m) P1_MM(1, n, m);
    MFMA_DONE();
    SBAR();
    STAMP(14);
    const float qs = sec == 0 ? QSCALE : 1.0f;
#pragma unroll
    for (int m = 0; m < 4; ++m) mr[m][1] *= qs;
#pragma unroll
    for (int n = 0; n < 4; ++n)
#pragma unroll
      for (int sub = 0; sub < 2; ++sub) {
        const f32x4 a = mr[sub][1] * (acc[n][sub] - mr[sub][0] * c4[n]), b = mr[sub + 2][1] * (acc[n][sub + 2] - mr[sub + 2][0] * c4[n]);
        unsigned w0 = pk_bf16(a[0], a[1]), w1 = pk_bf16(a[2], a[3]), w2 = pk_bf16(b[0], b[1]), w3 = pk_bf16(b[2], b[3]);
        asm volatile("s_mov_b64 exec, %4\n\ts_nop 0\n\tv_permlane16_swap_b32 %0, %2\n\tv_permlane16_swap_b32 %1, %3\n\ts_mov_b64 exec, -1"
                     : "+v"(w0), "+v"(w1), "+v"(w2), "+v"(w3) : "s"(swp[n]));
        st128((u32x4){w0, w1, w2, w3}, rsw, so[n] + sub * 256, 0, SC1);
      }
  } else {
    f32x4 mr[4][2];
    float c1[4];
#pragma unroll
    for (int i = 0; i < 4; ++i) {
      mr[i][0] = *LDSP(const f32x4, st + (wr * 64 + i * 16 + 4 * G) * 8);
      mr[i][1] = *LDSP(const f32x4, st + (wr * 64 + i * 16 + 4 * G) * 8 + 16);
      c1[i] = *LDSP(const float, st + 1024 + (wc * 64 + (i ^ (2 * wr)) * 16 + l15) * 4);
    }
#pragma unroll
    for (int n = 2; n < 4; ++n)
#pragma unroll
      for (int m = 0; m < 4; ++m) P1_MM(1, n, m);
    MFMA_DONE();
    SBAR();
    STAMP(14);
#pragma unroll
    for (int n = 0; n < 4; ++n)
#pragma unroll
      for (int t = 0; t < 2; ++t) {
        u32x2 w[2];
#pragma unroll
        for (int i = 0; i < 2; ++i) {
          const int m = 2 * t + i;
          const f32x4& v = acc[n][m];
          w[i] = (u32x2){pk_bf16(mr[m][0][1] * (v[0] - mr[m][0][0] * c1[n]), mr[m][0][3] * (v[1] - mr[m][0][2] * c1[n])),
                         pk_bf16(mr[m][1][1] * (v[2] - mr[m][1][0] * c1[n]), mr[m][1][3] * (v[3] - mr[m][1][2] * c1[n]))};
        }
        st128(widen32(w[0], w[1]), rsw, so[n] ^ (t * 64), 0, SC1);
      }
  }
#undef P1_MM
#undef P1_CS
#undef P1_RX
#undef P1_RW
#undef P1_H1
#undef P1_H2
}

constexpr int CHUNK_B = 4 * KVP_B;
constexpr int L_SCR = 3 * CHUNK_B;
constexpr int L_Q = L_SCR + 4 * 8192;
constexpr int L_WO = 163840 - 100 * 1024;
constexpr int L_B4 = 0, B4_B = 28 * 1024;
constexpr int L_A4 = 3 * B4_B, A4_B = 16 * 1024;
constexpr int L_CS = L_A4 + 3 * A4_B;
constexpr int L_TK = L_CS + 1024;
constexpr int L_A5 = 12 * 10240;
constexpr int L_XCH = 0;

__device__ __forceinline__ float max16(const f32x16& s) {
  float a = fmaxf(fmaxf(s[0], s[1]), s[2]), b = fmaxf(fmaxf(s[3], s[4]), s[5]);
  a = fmaxf(fmaxf(a, s[6]), s[7]); b = fmaxf(fmaxf(b, s[8]), s[9]);
  a = fmaxf(fmaxf(a, s[10]), s[11]); b = fmaxf(fmaxf(b, s[12]), s[13]);
  a = fmaxf(fmaxf(a, s[14]), s[15]);
  a = fmaxf(a, b);
  auto r = __builtin_amdgcn_permlane32_swap(__float_as_uint(a), __float_as_uint(a), false, false);
  return fmaxf(__uint_as_float(r[0]), __uint_as_float(r[1]));
}
__device__ __forceinline__ bf16x8 pack8(const f32x16& s, int o) {
  u32x4 w = {pk_bf16(s[o], s[o + 1]), pk_bf16(s[o + 2], s[o + 3]), pk_bf16(s[o + 4], s[o + 5]), pk_bf16(s[o + 6], s[o + 7])};
  return BC8(w);
}
__device__ __forceinline__ f32x16 lo16(const f32x32& a) { return __builtin_shufflevector(a, a, 0, 1, 2, 3, 4, 5, 6, 7, 8, 9, 10, 11, 12, 13, 14, 15); }
__device__ __forceinline__ f32x16 hi16(const f32x32& a) { return __builtin_shufflevector(a, a, 16, 17, 18, 19, 20, 21, 22, 23, 24, 25, 26, 27, 28, 29, 30, 31); }
__device__ __forceinline__ bf16x4 lo4(bf16x8 a) { return __builtin_shufflevector(a, a, 0, 1, 2, 3); }
__device__ __forceinline__ bf16x4 hi4(bf16x8 a) { return __builtin_shufflevector(a, a, 4, 5, 6, 7); }

template <typename R>
__device__ __forceinline__ void attn_slow(R rsw, int kv, bf16x8 q1a, bf16x8 q1b, bf16x4 q2, int lane, f32x16& oa, f32x16& ob) {
  const int l31 = lane & 31, hi = lane >> 5;
  oa = (f32x16){};
  ob = (f32x16){};
  float ma = -INFINITY, mb = -INFINITY;
#pragma unroll 1
  for (int t = 0; t < 64; ++t) {
    const int p = kv + (t >> 1) * KVP_B;
    const bf16x8 k1 = BC8(__builtin_amdgcn_raw_buffer_load_b128(rsw, lane * 16, p + (t & 1) * 1024, SC1));
    const bf16x4 k2 = __builtin_bit_cast(bf16x4, __builtin_amdgcn_raw_buffer_load_b64(rsw, l31 * 16, p + 2048 + (t & 1) * 8, SC1));
    const int d = l31 > 21 ? 21 : l31;
    const unsigned vo = 2560 + d * 128 + 16 * (((t & 1) * 4 + hi) ^ ((d >> 1) & 7));
    const bf16x8 v1 = BC8(__builtin_amdgcn_raw_buffer_load_b128(rsw, vo, p, SC1));
    const bf16x8 v2 = BC8(__builtin_amdgcn_raw_buffer_load_b128(rsw, vo ^ 32, p, SC1));
    const f32x32 s2 = MFMA2B(k2, q2, ((f32x32){}));
    f32x16 sa = MFMA32(k1, q1a, lo16(s2)), sb = MFMA32(k1, q1b, hi16(s2));
    const float na = fmaxf(ma, max16(sa)), nb = fmaxf(mb, max16(sb));
    const float fa = __builtin_amdgcn_exp2f(ma - na), fb = __builtin_amdgcn_exp2f(mb - nb);
    ma = na;
    mb = nb;
#pragma unroll
    for (int r = 0; r < 16; ++r) {
      sa[r] = __builtin_amdgcn_exp2f(sa[r] - na); oa[r] *= fa;
      sb[r] = __builtin_amdgcn_exp2f(sb[r] - nb); ob[r] *= fb;
    }
    oa = MFMA32(v1, pack8(sa, 0), oa);
    oa = MFMA32(v2, pack8(sa, 8), oa);
    ob = MFMA32(v1, pack8(sb, 0), ob);
    ob = MFMA32(v2, pack8(sb, 8), ob);
  }
}
__device__ __forceinline__ float row_sum(const f32x16& o) {
  auto r = __builtin_amdgcn_permlane32_swap(__float_as_uint(o[8]), __float_as_uint(o[8]), false, false);
  return __uint_as_float(r[1]);
}
__device__ __forceinline__ bool unsafe_rows(const f32x16& o) {
  float chk = 0.f;
#pragma unroll
  for (int i = 0; i < 12; ++i) chk += fabsf(o[i]);
  return !(chk < 0x1p100f);
}
__device__ __forceinline__ int blk64(int rt, int cb) { return (rt >> 2) | ((rt & 3) << 3) | (cb << 5); }
__device__ __forceinline__ int blk128(int rt, int cb) { return ((rt >> 3) << 2) | (cb >> 2) | ((rt & 7) << 3) | ((cb & 3) << 6); }
__device__ __forceinline__ int p3_ks(int i, int cb) { return i < 16 ? ((i + 2 * cb) & 15) : 16 + ((i + cb) & 3); }
__device__ __forceinline__ int p5_pair(int i, int cb) { const int r = i + cb * ROT5; return r >= 27 ? r - 27 : r; }
__device__ __forceinline__ unsigned unit_off(int U) { return (U >> 3) * 1024 + ((U >> 1) & 3) * 256 + (U & 1) * 8; }

__device__ __forceinline__ void role_a(lchar* lds, int wave, int lane, const char* __restrict__ x, char* ws) {
  STAMP_DECL;
  STAMP(0);
  const auto rsw = __builtin_amdgcn_make_buffer_rsrc(ws, 0, WS_END, 0x00020000);
  const int l31 = lane & 31, hi = lane >> 5;
  {
    const int row = blockIdx.x * 8 + wave * 2 + hi;
    u32x2 v[5];
#pragma unroll
    for (int i = 0; i < 5; ++i) v[i] = *(const u32x2*)(x + (size_t)row * 1280 + (i * 32 + l31) * 8);
    if (wave == 0 && lane < 24) __hip_atomic_store(counter(ws, blockIdx.x, lane), 0u, __ATOMIC_RELAXED, __HIP_MEMORY_SCOPE_AGENT);
    if (wave == 1) {
      const unsigned fill = (lane & 8) ? 0u : 0x3f803f80u;
      st128((u32x4){fill, fill, fill, fill}, rsw, (blockIdx.x * 4 + (lane >> 4)) * KVP_B + (lane & 15) * 16, WS_KV + 2560 + 20 * 128, SC1);
    }
    auto stats = [&]() {
      auto sum32 = [](float t) {
        t += __uint_as_float(__builtin_amdgcn_update_dpp(0, __float_as_uint(t), 0xB1, 0xf, 0xf, true));
        t += __uint_as_float(__builtin_amdgcn_update_dpp(0, __float_as_uint(t), 0x4E, 0xf, 0xf, true));
        t += __uint_as_float(__builtin_amdgcn_update_dpp(0, __float_as_uint(t), 0x141, 0xf, 0xf, true));
        t += __uint_as_float(__builtin_amdgcn_update_dpp(0, __float_as_uint(t), 0x140, 0xf, 0xf, true));
        return swap16_add(t);
      };
      float t = 0.f;
#pragma unroll
      for (int i = 0; i < 5; ++i) t += (bf_lo(v[i][0]) + bf_hi(v[i][0])) + (bf_lo(v[i][1]) + bf_hi(v[i][1]));
      const float mu = sum32(t) * (1.0f / DM);
      t = 0.f;
#pragma unroll
      for (int i = 0; i < 5; ++i) {
        const float a = bf_lo(v[i][0]) - mu, b = bf_hi(v[i][0]) - mu, c = bf_lo(v[i][1]) - mu, d = bf_hi(v[i][1]) - mu;
        t += (a * a + b * b) + (c * c + d * d);
      }
      const float rstd = __builtin_amdgcn_rsqf(sum32(t) * (1.0f / DM) + LN_EPS);
      if (l31 == 0) __builtin_amdgcn_raw_buffer_store_b64((u32x2){__float_as_uint(mu), __float_as_uint(rstd)}, rsw, row * 8, WS_RS, SC1);
    };
    const int ti = (blockIdx.x & 1) * 30 + (blockIdx.x >> 3), rt = ((blockIdx.x & 7) >> 1) * 4 + (ti & 3), ct = blockIdx.x >= 240 ? 15 : ti >> 2;
    if (ct >= 15) {
      stats();
      WAITV_BARRIER(0);
    } else {
      if (ct >= 10) qkv_tile<true>(lds, wave, lane, rt, ct, rsw, stats STAMP_PASS);
      else qkv_tile<false>(lds, wave, lane, rt, ct, rsw, stats STAMP_PASS);
      STAMP(15);
      WAITV_BARRIER(0);
    }
  }
  {
    const int head = (blockIdx.x & 7) * 4 + (blockIdx.x >> 6), pair = ((blockIdx.x >> 3) & 7) * 4 + wave;
    const lchar* ka = lds + lane * 16;
    const lchar* kb = lds + 2048 + l31 * 16;
    const lchar* va[4];
    {
      const int d = l31 > 21 ? 21 : l31;
#pragma unroll
      for (int i = 0; i < 4; ++i) va[i] = lds + 2560 + d * 128 + 16 * ((2 * i + hi) ^ ((d >> 1) & 7));
    }
#define KOFF(t) (((t) >> 1) * KVP_B + ((t) & 1) * 1024)
#define VRD(t, hf) *LDSP(const bf16x8, va[((t) & 1) * 2 + (hf)] + ((t) >> 1) * KVP_B)
    BARRIER();
    STAMP(1);
    const lchar* const qp = lds + L_Q + wave * QP_B;
    const bf16x8 q1a = *LDSP(const bf16x8, qp + lane * 16), q1b = *LDSP(const bf16x8, qp + 1024 + lane * 16);
    const bf16x4 q2 = *LDSP(const bf16x4, qp + 2048 + l31 * 16 + hi * 8);
    bf16x8 k1, k2, v1, v2 = {0, 0, 0, 0, 0, 0, 0, 0};
    u32x4 pa, pb = {0, 0, 0, 0};
    f32x32 negm;
    f32x16 sa[2], sb[2], oa = {}, ob = {};
    {
      k2 = *LDSP(const bf16x8, kb);
      k1 = *LDSP(const bf16x8, ka);
      v1 = VRD(0, 0);
      const f32x32 s2 = MFMA2B(lo4(k2), q2, ((f32x32){}));
      sa[0] = MFMA32(k1, q1a, lo16(s2));
      sb[0] = MFMA32(k1, q1b, hi16(s2));
      k1 = *LDSP(const bf16x8, ka + KOFF(1));
      const float ma = -max16(sa[0]), mb = -max16(sb[0]);
#pragma unroll
      for (int r = 0; r < 16; ++r) { negm[r] = ma; negm[16 + r] = mb; sa[0][r] += ma; sb[0][r] += mb; }
    }
#define EX(S, r) S[r] = __builtin_amdgcn_exp2f(S[r])
#define G1(S) do { EX(S, 0); EX(S, 1); EX(S, 2); EX(S, 3); EX(S, 4); PIN(S); SBAR(); } while (0)
#define G2(S, P) do { EX(S, 5); SBAR(); P[0] = pk_bf16(S[0], S[1]); SBAR(); EX(S, 6); SBAR(); P[1] = pk_bf16(S[2], S[3]); SBAR(); \
                      EX(S, 7); SBAR(); P[2] = pk_bf16(S[4], S[5]); SBAR(); P[3] = pk_bf16(S[6], S[7]); PIN(P); SBAR(); } while (0)
#define G3(S) do { EX(S, 8); EX(S, 9); EX(S, 10); EX(S, 11); EX(S, 12); PIN(S); SBAR(); } while (0)
#define G4(S, P) do { EX(S, 13); SBAR(); P[0] = pk_bf16(S[8], S[9]); SBAR(); EX(S, 14); SBAR(); P[1] = pk_bf16(S[10], S[11]); SBAR(); \
                      EX(S, 15); SBAR(); P[2] = pk_bf16(S[12], S[13]); SBAR(); P[3] = pk_bf16(S[14], S[15]); PIN(P); SBAR(); } while (0)
    int slot = 0;
#pragma unroll 1
    for (int c = 0; c < 8; ++c) {
      const int adv = (slot == 2) ? -2 * CHUNK_B : CHUNK_B;
      slot = (slot == 2) ? 0 : slot + 1;
#pragma unroll
      for (int j = 0; j < 8; ++j) {
        const int cur = j & 1, nxt = cur ^ 1;
        if (j == 6) {
          if (c < 7) asm volatile("s_barrier" ::: "memory");
          ka += adv;
          kb += adv;
        }
        SBAR();
        G1(sa[cur]);
        { const f32x32 t = MFMA2B(cur ? lo4(k2) : hi4(k2), q2, negm); sa[nxt] = lo16(t); sb[nxt] = hi16(t); }
        SBAR();
        if (!cur) { k2 = *LDSP(const bf16x8, kb + (((j + 2) & 7) >> 1) * KVP_B); SBAR(); }
        G2(sa[cur], pa);
        G3(sa[cur]);
        ob = MFMA32(v2, BC8(pb), ob); SBAR();
        v2 = VRD(j, 1); SBAR();
        G4(sa[cur], pb);
        oa = MFMA32(v1, BC8(pa), oa); SBAR();
        G1(sb[cur]);
        sa[nxt] = MFMA32(k1, q1a, sa[nxt]); SBAR();
        G2(sb[cur], pa);
        sb[nxt] = MFMA32(k1, q1b, sb[nxt]); SBAR();
        k1 = *LDSP(const bf16x8, ka + KOFF((j + 2) & 7)); SBAR();
        G3(sb[cur]);
        oa = MFMA32(v2, BC8(pb), oa); SBAR();
        G4(sb[cur], pb);
        ob = MFMA32(v1, BC8(pa), ob); SBAR();
        if (j == 7) { va[0] += adv; va[1] += adv; va[2] += adv; va[3] += adv; }
        v1 = VRD((j + 1) & 7, 0); SBAR();
      }
    }
    ob = MFMA32(v2, BC8(pb), ob);
#undef EX
#undef G1
#undef G2
#undef G3
#undef G4
#undef KOFF
#undef VRD
    STAMP(2);
    if (__builtin_expect(__any(unsafe_rows(oa) || unsafe_rows(ob)), 0)) attn_slow(rsw, WS_KV + head * 32 * KVP_B, q1a, q1b, q2, lane, oa, ob);
    const float ra = __builtin_amdgcn_rcpf(row_sum(oa)), rb = __builtin_amdgcn_rcpf(row_sum(ob));
    u32x2 wa[3], wb[3];
#pragma unroll
    for (int g = 0; g < 3; ++g) {
      wa[g] = (u32x2){pk_bf16(oa[4 * g] * ra, oa[4 * g + 1] * ra), pk_bf16(oa[4 * g + 2] * ra, oa[4 * g + 3] * ra)};
      wb[g] = (u32x2){pk_bf16(ob[4 * g] * rb, ob[4 * g + 1] * rb), pk_bf16(ob[4 * g + 2] * rb, ob[4 * g + 3] * rb)};
    }
    const int ob_ = WS_O + (head * 32 + pair) * QP_B;
    st128(widen32(wa[0], wb[0]), rsw, hi * 1024 + l31 * 16, ob_, SC1);
    st128(widen32(wa[1], wb[1]), rsw, hi * 1024 + l31 * 16 + 512, ob_, SC1);
    {
      const u32x4 r = widen32(wa[2], wb[2]);
      __builtin_amdgcn_raw_buffer_store_b64((u32x2){r[0], r[1]}, rsw, 2048 + lane * 8, ob_, SC1);
    }
    BARRIER();
    DRAIN();
    if (lane < 8) signal_add(counter(ws, blk64(pair, lane), C_O + (head & 3)));
    STAMP(3);
  }
  BARRIER();
  {
    const int rt = ((blockIdx.x & 7) >> 2) * 8 + ((blockIdx.x >> 3) & 7), cb = (blockIdx.x & 3) * 4 + (blockIdx.x >> 6);
    const int wr = wave >> 1, wc = wave & 1, G = lane >> 4;
    f32x4 acc[7][4] = {};
    bf16x8 af[2][4], bf[7];
    const lchar* ab = lds + L_A4 + wr * 8 * 1024 + lane * 16;
    const lchar* bb = lds + L_B4 + wc * 7 * 1024 + lane * 16;
    const lchar* bb2 = bb + 2 * B4_B;
    PIN(ab); PIN(bb); PIN(bb2);
#pragma unroll
    for (int n = 0; n < 7; ++n)
#pragma unroll
      for (int m = 0; m < 4; ++m) PIN(acc[n][m]);
#define K4_B(sl) ((sl) == 2 ? bb2 : bb + (sl) * B4_B)
#pragma unroll
    for (int n = 0; n < 7; ++n) bf[n] = *LDSP(const bf16x8, bb + n * 1024);
    STAMP(4);
    BARRIER();
    STAMP(5);
#pragma unroll
    for (int m = 0; m < 4; ++m) af[0][m] = *LDSP(const bf16x8, ab + 2 * m * 1024);
    f32x4 cs[7], tk[4];
#define K4_MM(buf, n, m) MFMA16_ACC(acc[n][m], bf[n], af[buf][m])
#define K4_H1(sl)                                                                     \
  _Pragma("unroll") for (int n = 0; n < 7; ++n) {                                     \
    SBAR(); K4_MM(0, n, 0); K4_MM(0, n, 1); SBAR();                                   \
    if (n < 4) af[1][n] = *LDSP(const bf16x8, ab + (sl) * A4_B + (2 * n + 1) * 1024); \
    SBAR(); K4_MM(0, n, 2); K4_MM(0, n, 3); SBAR();                                   \
    bf[n] = *LDSP(const bf16x8, K4_B(sl) + (14 + n) * 1024);                          \
  }
#define K4_H2(sl)                                                                     \
  _Pragma("unroll") for (int n = 0; n < 7; ++n) {                                     \
    SBAR(); K4_MM(1, n, 0); K4_MM(1, n, 1); SBAR();                                   \
    if (n >= 2 && n < 6) af[0][n - 2] = *LDSP(const bf16x8, ab + (sl) * A4_B + 2 * (n - 2) * 1024); \
    SBAR(); K4_MM(1, n, 2); K4_MM(1, n, 3); SBAR();                                   \
    if (n == 1) { BARRIER(); bf[0] = *LDSP(const bf16x8, K4_B(sl)); }                 \
    if (n >= 1) bf[n] = *LDSP(const bf16x8, K4_B(sl) + n * 1024);                     \
  }
    __builtin_amdgcn_s_waitcnt(0xC07F);
#pragma unroll 1
    for (int s = 0; s < 3; ++s) {
      K4_H1(0); K4_H2(1);
      K4_H1(1); K4_H2(2);
      K4_H1(2); K4_H2(0);
    }
    K4_H1(0);
#pragma unroll
    for (int n = 0; n < 7; ++n) cs[n] = *LDSP(const f32x4, lds + L_CS + ((wc * 7 + n) * 16 + G * 4) * 4);
#pragma unroll
    for (int m = 0; m < 4; ++m) tk[m] = *LDSP(const f32x4, lds + L_TK + (wr * 64 + m * 16 + (lane & 15)) * 16);
#pragma unroll
    for (int n = 0; n < 7; ++n) {
      SBAR(); K4_MM(1, n, 0); K4_MM(1, n, 1); K4_MM(1, n, 2); K4_MM(1, n, 3); SBAR();
      if (n == 2) BARRIER();
    }
    MFMA_DONE();
    SBAR();
#undef K4_B
#undef K4_H1
#undef K4_H2
#undef K4_MM
    STAMP(6);
    const int f0 = (cb * 2 + wc) * 56;
    auto geglu = [&](const f32x4& g, const f32x4& u, const f32x4& cg, const f32x4& cu, const f32x4& t) -> u32x2 {
      const f32x4 gg = g + t[0] * cg, uu = u + t[0] * cu;
      const f32x4 z = gg * (t[1] + t[2] * (gg * gg));
      f32x4 e, r;
#pragma unroll
      for (int j = 0; j < 4; ++j) e[j] = __builtin_amdgcn_exp2f(z[j]);
      e = t[3] + e * t[3];
#pragma unroll
      for (int j = 0; j < 4; ++j) r[j] = __builtin_amdgcn_rcpf(e[j]);
      const f32x4 o = gg * uu * r;
      return (u32x2){pk_bf16(o[0], o[1]), pk_bf16(o[2], o[3])};
    };
    const int abase = WS_A + (rt * 8 + wr * 4) * 54 * 1024;
    if (f0 < FF) {
#pragma unroll
      for (int n = 0; n < 3; ++n) {
        const unsigned off = (G & 1) * 54 * 1024 + unit_off((f0 >> 2) + n * 4 + (G & 2)) + (lane & 15) * 16;
#pragma unroll
        for (int mp = 0; mp < 2; ++mp) {
          const u32x2 r0 = geglu(acc[n][2 * mp], acc[n + 3][2 * mp], cs[n], cs[n + 3], tk[2 * mp]);
          const u32x2 r1 = geglu(acc[n][2 * mp + 1], acc[n + 3][2 * mp + 1], cs[n], cs[n + 3], tk[2 * mp + 1]);
          st128(widen16(r0, r1), rsw, off, abase + mp * 2 * 54 * 1024, SC1);
        }
      }
    }
    if (f0 + 48 < FF) {
      f32x4 cg, cu;
#pragma unroll
      for (int j = 0; j < 4; ++j) {
        auto r = __builtin_amdgcn_permlane32_swap(__float_as_uint(cs[6][j]), __float_as_uint(cs[6][j]), false, false);
        cg[j] = __uint_as_float(r[0]);
        cu[j] = __uint_as_float(r[1]);
      }
      u32x2 r2[2];
#pragma unroll
      for (int mp = 0; mp < 2; ++mp) {
        f32x4 g, u;
#pragma unroll
        for (int j = 0; j < 4; ++j) {
          auto r = __builtin_amdgcn_permlane32_swap(__float_as_uint(acc[6][2 * mp][j]), __float_as_uint(acc[6][2 * mp + 1][j]), false, false);
          g[j] = __uint_as_float(r[0]);
          u[j] = __uint_as_float(r[1]);
        }
        f32x4 t;
#pragma unroll
        for (int j = 0; j < 4; ++j) t[j] = hi ? tk[2 * mp + 1][j] : tk[2 * mp][j];
        r2[mp] = geglu(g, u, cg, cu, t);
      }
      st128(widen16(r2[0], r2[1]), rsw, (2 * (G & 1) + hi) * 54 * 1024 + unit_off((f0 >> 2) + 12) + (lane & 15) * 16, abase, SC1);
    }
    STAMP(7);
    DRAIN();
    if (lane < 8) signal_add(counter(ws, blk64(rt * 2 + wr, lane), C_A + (cb & 3)));
    STAMP(8);
  }
  {
    const unsigned lbase = (unsigned)(uintptr_t)lds;
    const int rt = (blockIdx.x & 7) * 4 + ((blockIdx.x >> 3) & 3), cb = blockIdx.x >> 5;
    const char* const ap = ws + WS_A + (rt * 4 + wave) * 54 * 1024;
    const char* const wp = ws + WS_WD + cb * 270 * 1024;
    int ao = cb * ROT5 * 2048, as = 0;
    int wo = p5_pair(16, cb) * 10240 + wave * 2048, wl = wave * 2048;
    auto issue_a = [&]() {
      dma_run<2>(ap + ao, lane * 16, lbase + L_A5 + as + wave * 2048);
      ao = ao == 26 * 2048 ? 0 : ao + 2048;
      as = as == 4 * 8192 ? 0 : as + 8192;
    };
    BARRIER();
    BARRIER();
    wait_count(counter(ws, blockIdx.x, C_A + (lane & 3)), 8);
    STAMP(9);
    issue_a(); issue_a(); issue_a(); issue_a();
#pragma unroll 1
    for (int s = 0; s < 6; ++s) {
      WAITV_BARRIER(6);
      issue_a();
    }
#pragma unroll 1
    for (int s = 6; s < 20; ++s) {
      WAITV_BARRIER(6);
      issue_a();
      dma_run<2>(wp + wo, lane * 16, lbase + wl);
      wo = wo + 8192 >= 270 * 1024 ? wo + 8192 - 270 * 1024 : wo + 8192;
      wl += 8192;
    }
#pragma unroll 1
    for (int s = 20; s < 23; ++s) {
      WAITV_BARRIER(6);
      issue_a();
    }
    WAITV_BARRIER(6);
    WAITV_BARRIER(4);
    WAITV_BARRIER(2);
    WAITV_BARRIER(0);
  }
  STAMP_OUT(ws, 1);
}

__device__ __forceinline__ void role_b(lchar* lds, int lw, int lane, const char* __restrict__ x, const char* __restrict__ wqkv, const char* __restrict__ wo,
                                       const char* __restrict__ wg, const char* __restrict__ wu, const char* __restrict__ wd,
                                       char* ws, char* __restrict__ out) {
  STAMP_DECL;
  STAMP(0);
  const unsigned lbase = (unsigned)(uintptr_t)lds;
  {
    u64 tag;
    const int ti = (blockIdx.x & 1) * 30 + (blockIdx.x >> 3), rt = ((blockIdx.x & 7) >> 1) * 4 + (ti & 3), ct = blockIdx.x >= 240 ? 15 : ti >> 2;
    auto code_to_l2 = [&]() {
      u64 c0, c1;
      asm volatile("s_getpc_b64 s[20:21]\n\ts_add_u32 s20, s20, block_fwd@rel32@lo+4\n\ts_addc_u32 s21, s21, block_fwd@rel32@hi+12" : "={s[20:21]}"(c0)::"scc");
      asm volatile("s_getpc_b64 s[22:23]\n\ts_add_u32 s22, s22, block_end@rel32@lo+4\n\ts_addc_u32 s23, s23, block_end@rel32@hi+12" : "={s[22:23]}"(c1)::"scc");
      const unsigned len = (unsigned)(c1 - c0), o = (((blockIdx.x >> 3) & 1) * 4 + lw) * 8192;
      if (len < 65536 && o < len) touch64((const void*)c0, min(o + lane * 128, len), lbase + L_RS + 1536);
    };
    if (ct >= 15) {
      code_to_l2();
      tag = load_tag(ws);
      WAITV_BARRIER(0);
      if (lw == 0 && lane == 0) flag_store(ws + WS_FS + blockIdx.x * 8, tag);
      if (lw == 3) wait_started(ws, tag, lane);
    } else {
      const unsigned xv = (lane >> 3) * 1280 + 16 * ((lane & 7) ^ ((4 * lw + (lane >> 4)) & 7));
      const unsigned wv = (lane >> 4) * 3840 + 16 * ((lane & 15) ^ (((lane >> 4) << 2) | lw));
      const char* const xb = x + (size_t)(rt * 128 + lw * 8) * 1280;
      const char* const wb = wqkv + (size_t)(lw * 4) * 3840 + ct * 256;
      int ks = (ct + (rt * 5 >> 1)) % 10;
      int slot = lw * 1024;
      auto issue = [&]() {
#pragma unroll
        for (int i = 0; i < 4; ++i) {
          dma16(xb + i * 32 * 1280 + ks * 128, xv, lbase + L_X1 + slot + i * 4096);
          dma16(wb + (size_t)(ks * 64 + i * 16) * 3840, wv, lbase + L_W1 + slot + i * 4096);
        }
        ks = ks == 9 ? 0 : ks + 1;
        slot = slot >= 32768 ? slot - 32768 : slot + 16384;
      };
      issue();
      issue();
      tag = load_tag(ws);
      STAMP(12);
      WAITV_BARRIER(8);
      issue();
      WAITV_BARRIER(8);
      if (lw == 0 && lane == 0) flag_store(ws + WS_FS + blockIdx.x * 8, tag);
#pragma unroll 1
      for (int s = 2; s < 9; ++s) {
        issue();
        if (s == 7 && lw == 3) {
          wait_started(ws, tag, lane);
          if (blockIdx.x == 0 && lane == 0) flag_store(ws + WS_SALT, tag);
          dma_run<1>(ws + WS_RS + rt * 1024, lane * 16, lbase + L_RS);
        }
        WAITV_BARRIER(8);
      }
      WAITV_BARRIER(0);
      BARRIER();
      const int sec = ct / 5, c0 = (ct - sec * 5) * 128, hh = c0 / 20 + (lane >> 3), qb = lane & 7;
      unsigned* const cnt = counter(ws, (hh >> 2) | (qb << 3) | ((hh & 3) << 6), sec == 0 ? C_Q : C_KV + (rt >> 1));
      const bool send = lw == 3 && hh * 20 < c0 + 128 && (sec != 0 || qb == (rt >> 1));
      BARRIER();
      if (send) signal_add(cnt);
    }
  }
  STAMP(13);
  const auto rsw = __builtin_amdgcn_make_buffer_rsrc(ws, 0, WS_END, 0x00020000);
  const int rt = (blockIdx.x & 7) * 4 + ((blockIdx.x >> 3) & 3), cb = blockIdx.x >> 5;
  const int rt4 = ((blockIdx.x & 7) >> 2) * 8 + ((blockIdx.x >> 3) & 7), cb4 = (blockIdx.x & 3) * 4 + (blockIdx.x >> 6);
  const int rh = lw & 1, kp = lw >> 1, tt = rh * 2 + kp, G = lane >> 4;
  const int tok = (rt * 4 + tt) * 16 + (lane & 15);
  u32x2 res[5];
  float csum = 0.f;
  {
    const int head = (blockIdx.x & 7) * 4 + (blockIdx.x >> 6), qblk = (blockIdx.x >> 3) & 7;
    const char* const kv = ws + WS_KV + head * 32 * KVP_B + lw * 7168;
    const unsigned sides = (head * 20) / 128 == (head * 20 + 19) / 128 ? 1 : 2;
    int ch = ROT2 ? qblk : 0, slot = 0;
    auto issue = [&]() {
      if (lw < 3) dma_lin<7>(kv + ch * CHUNK_B, lane * 16, lbase + slot + lw * 7168);
      ch = (ch + 1) & 7;
      slot = (slot == 2 * CHUNK_B) ? 0 : slot + CHUNK_B;
    };
    if (lw == 3) {
      wait_count(counter(ws, blockIdx.x, C_Q), 2 * sides);
      dma_lin<10>(ws + WS_Q + (head * 32 + qblk * 4) * QP_B, lane * 16, lbase + L_Q);
    } else {
      wait_count(counter(ws, blockIdx.x, C_KV + ch), 4 * sides);
    }
    issue();
    STAMP(14);
    WAITV_BARRIER(0);
    if (lw < 3) wait_count(counter(ws, blockIdx.x, C_KV + (lane & 7)), 4 * sides);
    issue();
    issue();
    const int unit = blockIdx.x * 4 + lw;
    const bool gu = unit < 620;
    int uks, uc;
    {
      const char *s0, *s1;
      unsigned voff;
      int pitch, r0, r1, r2;
      if (gu) {
        uks = unit / 31; uc = unit - uks * 31; pitch = FF * 2;
        s0 = wg + uc * 112; s1 = wu + uc * 112;
        voff = (lane >> 3) * pitch + min(lane & 7, uc == 30 ? 5 : 6) * 16;
        r0 = uks * 32; r1 = 8; r2 = 16;
      } else {
        const int u = unit < 890 ? unit - 620 : unit < 990 ? unit - 890 : 0;
        uks = u / 5; uc = u - uks * 5; pitch = DM * 2;
        s0 = (unit < 890 ? wd : wo) + uc * 256; s1 = s0 + 128;
        int row = lane >> 3;
        if (unit < 890) { r0 = uks * 32; r1 = 8; r2 = 16; }
        else if (uks < 16) { r0 = uks * 40; r1 = 8; r2 = 20; }
        else { r0 = (uks - 16) * 160 + 16; r1 = 40; r2 = 80; row = (row >> 2) * 20 + (row & 3); }
        voff = row * pitch + (lane & 7) * 16;
      }
#pragma unroll
      for (int i = 0; i < 4; ++i) {
        dma16(s0 + (size_t)(r0 + (i & 1) * r1 + (i >> 1) * r2) * pitch, voff, lbase + L_SCR + lw * 8192 + i * 1024);
        dma16(s1 + (size_t)(r0 + (i & 1) * r1 + (i >> 1) * r2) * pitch, voff, lbase + L_SCR + lw * 8192 + 4096 + i * 1024);
      }
    }
    WAITV_BARRIER(15);
    WAITV_BARRIER(8);
    issue();
    WAITV_BARRIER(0);
    issue();
    {
      const int p = lane & 3;
      const lchar* const sp = lds + L_SCR + lw * 8192 + (8 * G + ((lane & 15) >> 2)) * 128 + p * 8;
      if (gu) {
        const bf16x8 ones = {0x3f80, 0x3f80, 0x3f80, 0x3f80, 0x3f80, 0x3f80, 0x3f80, 0x3f80};
        const int dst = WS_WGU + (((uc >> 1) * 20 + uks) * 14 + (uc & 1) * 7) * 1024;
        float c0 = 0.f, c1 = 0.f;
#pragma unroll
        for (int t = 0; t < 7; ++t) {
          const lchar* const q = t < 3 ? sp + t * 32 : t < 6 ? sp + 4096 + (t - 3) * 32 : sp + 96 + (p >> 1) * (4096 - 16);
          const bf16x8 fr = cat8(tr_read(q), tr_read(q + 512));
          st128(__builtin_bit_cast(u32x4, fr), rsw, lane * 16, dst + t * 1024, SC1);
          const f32x4 cs = MFMA16(ones, fr, ((f32x4){0.f, 0.f, 0.f, 0.f}));
          if (t < 4) c0 = (G == t) ? cs[0] : c0;
          else c1 = (G == t - 4) ? cs[0] : c1;
        }
        const int cp = WS_CSP + (uks * 3584 + uc * 112) * 4;
        __builtin_amdgcn_raw_buffer_store_b32(__float_as_uint(c0), rsw, lane * 4, cp, SC1);
        if (lane < 48) __builtin_amdgcn_raw_buffer_store_b32(__float_as_uint(c1), rsw, lane * 4 + 256, cp, SC1);
      } else if (unit < 990) {
        const int dst = unit < 890 ? WS_WD : WS_WO, nks = unit < 890 ? 54 : 20;
#pragma unroll
        for (int t = 0; t < 8; ++t) {
          const lchar* const q = sp + (t >> 2) * 4096 + (t & 3) * 32;
          const bf16x8 fr = cat8(tr_read(q), tr_read(q + 512));
          const int t16 = uc * 8 + t, c5 = (t16 * 205) >> 10, tile = t16 - c5 * 5;
          st128(__builtin_bit_cast(u32x4, fr), rsw, lane * 16, dst + ((c5 * nks + uks) * 5 + tile) * 1024, SC1);
        }
      }
    }
    WAITV_BARRIER(0);
    if (lw == 3 && lane == 0) signal_add(counter(ws, blockIdx.x & 7, C_W));
    issue();
#pragma unroll
    for (int n = 0; n < 5; ++n) res[n] = *(const u32x2*)(x + (size_t)tok * 1280 + (cb * 20 + n * 4 + G) * 8);
    WAITV_BARRIER(0);
    issue();
    if (lw == 3) wait_count(counter(ws, lane & 7, C_W), 32);
    WAITV_BARRIER(0);
    issue();
    const char* const wop = ws + WS_WO + cb * 100 * 1024;
    auto wo_step = [&](int i) { dma_lin<5>(wop + p3_ks(i, cb) * 5120, lane * 16, lbase + L_WO + i * 5120); };
    wo_step(4 + lw);
    wo_step(8 + lw);
    WAITV_BARRIER(10);
    wo_step(12 + lw);
    wo_step(16 + lw);
    wo_step(lw);
#pragma unroll
    for (int i = 0; i < 20; ++i)
      csum += __uint_as_float(__builtin_amdgcn_raw_buffer_load_b32(rsw, min(lane, 55) * 4, WS_CSP + (i * 3584 + cb4 * 224 + lw * 56) * 4, SC1));
    {
      const char* const p = ws + WS_WGU + (blockIdx.x & 3) * 4 * 280 * 1024 + (blockIdx.x >> 3) * 35840 + lw * 8960;
      touch64(p, lane * 128, lbase + 2 * CHUNK_B + 8192);
      touch64(p, min(8192 + lane * 128, 8832), lbase + 2 * CHUNK_B + 8192);
    }
    PIN(csum);
    STAMP(1);
    WAITV_BARRIER(0);
    STAMP(2);
  }
  const char* const wgu = ws + WS_WGU + cb4 * 280 * 1024;
  const int rot4 = ROT4 == 2 ? (blockIdx.x >> 6) * 5 >> 1 : ROT4 == 1 ? (blockIdx.x >> 7) * 5 : 0;
  {
    dma_lin<14>(wgu + rot4 * B4_B + lw * 14 * 1024, lane * 16, lbase + L_B4 + lw * 14 * 1024);
    const lchar* wb0 = lds + L_WO + lane * 16;
    const lchar* wb1 = wb0 + 40960;
    PIN(wb0); PIN(wb1);
    bf16x8 wf[8][5];
#pragma unroll
    for (int i = 0; i < 8; ++i)
#pragma unroll
      for (int n = 0; n < 5; ++n) wf[i][n] = *LDSP(const bf16x8, wb0 + i * 5120 + n * 1024);
    int so[20];
#pragma unroll
    for (int i = 0; i < 20; ++i) {
      so[i] = WS_O + ((i < 16 ? 2 * p3_ks(i, cb) : 8 * (p3_ks(i, cb) - 16)) * 32 + rt) * QP_B;
      PINS(so[i]);
    }
    unsigned vm = (G >> 1) * 32 * QP_B + (tt >> 1) * 1024 + (G & 1) * 512 + ((tt & 1) * 16 + (lane & 15)) * 16;
    unsigned vr = 2 * G * 32 * QP_B + 2048 + (tt * 16 + (lane & 15)) * 8, vr2 = vr + 32 * QP_B;
    f32x4 acc[5] = {};
    PIN(vm); PIN(vr); PIN(vr2);
#pragma unroll
    for (int n = 0; n < 5; ++n) PIN(acc[n]);
    DRAIN();
    __builtin_amdgcn_s_waitcnt(0xC07F);
    wait_count(counter(ws, blockIdx.x, C_O + (lane & 3)), 8);
    STAMP(3);
    constexpr int AW = 8;
    u32x4 a[AW];
    auto load_a = [&](int i) -> u32x4 {
      if (i < 16) return __builtin_amdgcn_raw_buffer_load_b128(rsw, vm, so[i], SC1);
      const u32x2 lo = __builtin_amdgcn_raw_buffer_load_b64(rsw, vr, so[i], SC1);
      const u32x2 hi2 = __builtin_amdgcn_raw_buffer_load_b64(rsw, vr2, so[i], SC1);
      return (u32x4){lo[0], lo[1], hi2[0], hi2[1]};
    };
#pragma unroll
    for (int i = 0; i < AW; ++i) { a[i] = load_a(i); SBAR(); }
#pragma unroll
    for (int i = 0; i < 20; ++i) {
#pragma unroll
      for (int n = 0; n < 5; ++n) {
        SBAR();
        MFMA16_ACC(acc[n], wf[i % 8][n], a[i % AW]);
        SBAR();
        const int j = (i - 1) * 4 + n;
        if (i >= 1 && n < 4 && j < 60) wf[(j / 5) % 8][j % 5] = *LDSP(const bf16x8, wb1 + j * 1024);
      }
      SBAR();
      if (i + AW < 20) a[i % AW] = load_a(i + AW);
    }
    MFMA_DONE();
    SBAR();
    STAMP(4);
    float s = 0.f, q = 0.f;
#pragma unroll
    for (int n = 0; n < 5; ++n) {
      const unsigned w0 = pk_bf16(acc[n][0] + bf_lo(res[n][0]), acc[n][1] + bf_hi(res[n][0]));
      const unsigned w1 = pk_bf16(acc[n][2] + bf_lo(res[n][1]), acc[n][3] + bf_hi(res[n][1]));
      res[n] = (u32x2){w0, w1};
      const float h0 = bf_lo(w0), h1 = bf_hi(w0), h2 = bf_lo(w1), h3 = bf_hi(w1);
      s += (h0 + h1) + (h2 + h3);
      q += (h0 * h0 + h1 * h1) + (h2 * h2 + h3 * h3);
    }
    const int hb = WS_H + (rt * 4 + tt) * 20480;
#pragma unroll
    for (int np = 0; np < 2; ++np)
      st128(widen16(res[2 * np], res[2 * np + 1]), rsw, unit_off(cb * 20 + (2 * np + (G & 1)) * 4 + (G & 2)) + (lane & 15) * 16, hb, SC1);
    __builtin_amdgcn_raw_buffer_store_b64(res[4], rsw, unit_off(cb * 20 + 16 + G) + (lane & 15) * 16, hb, SC1);
    s = swap32_add(swap16_add(s));
    q = swap32_add(swap16_add(q));
    if (lane < 16) __builtin_amdgcn_raw_buffer_store_b64((u32x2){__float_as_uint(s), __float_as_uint(q)}, rsw, tok * 8, WS_ST + cb * 16384, SC1);
    WAITV_BARRIER(0);
    if (lw == 0 && lane < 16) signal_add(counter(ws, blk128(rt >> 1, lane), C_H + (cb & 3)));
    STAMP(5);
  }
  {
    if (lane < 56) *LDSP(float, lds + L_CS + (lw * 56 + lane) * 4) = csum;
    dma_lin<7>(wgu + (rot4 + 2) * B4_B + lw * 7168, lane * 16, lbase + L_B4 + 2 * B4_B + lw * 7168);
    wait_count(counter(ws, blockIdx.x, C_H + (lane & 3)), 4);
    STAMP(6);
    const char* const ha = ws + WS_H + (rt4 * 8 + lw * 2) * 20480;
    int sa_ = 0, sb_ = 0;
    auto issue_a = [&](int s) {
      dma_run<2>(ha + s * 2048, lane * 16, lbase + L_A4 + sa_ + lw * 4096);
      dma_run<2>(ha + 20480 + s * 2048, lane * 16, lbase + L_A4 + sa_ + lw * 4096 + 2048);
      sa_ = (sa_ == 2 * A4_B) ? 0 : sa_ + A4_B;
    };
    issue_a(rot4);
    issue_a(rot4 + 1);
    u32x2 sp[4];
#pragma unroll
    for (int i = 0; i < 4; ++i)
      sp[i] = __builtin_amdgcn_raw_buffer_load_b64(rsw, ((lane >> 5) * 4 + i) * 16384 + (lane & 31) * 8, WS_ST + (rt4 * 128 + lw * 32) * 8, SC1);
    WAITV_BARRIER(8);
    issue_a(rot4 + 2);
    {
      constexpr float C0 = -2.0f * 0.7978845608028654f * 1.4426950408889634f, C1 = C0 * 0.044715f;
      const float sum = swap32_add((__uint_as_float(sp[0][0]) + __uint_as_float(sp[1][0])) + (__uint_as_float(sp[2][0]) + __uint_as_float(sp[3][0])));
      const float sq = swap32_add((__uint_as_float(sp[0][1]) + __uint_as_float(sp[1][1])) + (__uint_as_float(sp[2][1]) + __uint_as_float(sp[3][1])));
      const float mu = sum * (1.0f / DM), var = fmaxf(sq * (1.0f / DM) - mu * mu, 0.f) + LN_EPS, r = __builtin_amdgcn_rsqf(var);
      if (lane < 32) *LDSP(f32x4, lds + L_TK + (lw * 32 + lane) * 16) = (f32x4){-mu, C0 * r, C1 * r * r * r, var};
    }
    WAITV_BARRIER(4);
#pragma unroll 1
    for (int i = 3; i < 10; ++i) {
      const int s = rot4 + i >= 10 ? rot4 + i - 10 : rot4 + i;
      issue_a(s);
      dma_lin<7>(wgu + s * B4_B + lw * 7168, lane * 16, lbase + L_B4 + sb_ + lw * 7168);
      sb_ = (sb_ == 2 * B4_B) ? 0 : sb_ + B4_B;
      touch64(ws + WS_WD + cb * 270 * 1024 + (((blockIdx.x >> 3) & 3) * 4 + lw) * 17280, min((i - 3) * 20 + min(lane, 19), 134) * 128, lbase + L_TK + 2048);
      WAITV_BARRIER(12);
    }
    WAITV_BARRIER(0);
    BARRIER();
    STAMP(7);
  }
  {
    const char* const wdp = ws + WS_WD + cb * 270 * 1024;
#pragma unroll 1
    for (int g = 0; g < 4; ++g) {
      const char* const src = wdp + p5_pair(4 * g + lw, cb) * 10240;
      const unsigned dst = lbase + (g == 0 ? 12 + lw : 4 * g - 4 + lw) * 10240;
#pragma unroll
      for (int i = 0; i < 5; ++i) {
        dma_run<2>(src + i * 2048, lane * 16, dst + i * 2048);
        asm volatile("s_waitcnt vmcnt(%0)" ::"i"(PF_INFLIGHT) : "memory");
      }
    }
    STAMP(8);
    const lchar* const wb = lds + kp * 5120 + lane * 16;
    const lchar* const a0 = lds + L_A5 + (tt * 2 + kp) * 1024 + lane * 16;
    const lchar* const a1 = lds + L_A5 + ((tt ^ 1) * 2 + kp) * 1024 + lane * 16;
    bf16x8 wf[4][5], af[2][2];
    f32x4 acc[2][5] = {};
#pragma unroll
    for (int n = 0; n < 5; ++n) { PIN(acc[0][n]); PIN(acc[1][n]); }
    WAITV_BARRIER(0);
#pragma unroll
    for (int i = 0; i < 4; ++i)
#pragma unroll
      for (int n = 0; n < 5; ++n) wf[3 - i][n] = *LDSP(const bf16x8, wb + L_A5 + i * 10240 + n * 1024);
    __builtin_amdgcn_s_waitcnt(0xC07F);
    BARRIER();
    int ws_ = 0, as_ = 4 * 8192;
    const lchar *pw = wb, *p0 = a0 + as_, *p1 = a1 + as_;
#define P5_STEP(buf)                                                                        \
  do {                                                                                      \
    SBAR();                                                                                 \
    MFMA16_ACC(acc[0][0], wf[buf][0], af[buf][0]);                                          \
    wf[buf ^ 1][0] = *LDSP(const bf16x8, pw);                                               \
    SBAR();                                                                                 \
    MFMA16_ACC(acc[1][0], wf[buf][0], af[buf][1]);                                          \
    asm volatile("s_barrier" ::: "memory");                                                 \
    af[buf ^ 1][0] = *LDSP(const bf16x8, p0);                                               \
    SBAR();                                                                                 \
    MFMA16_ACC(acc[0][1], wf[buf][1], af[buf][0]);                                          \
    af[buf ^ 1][1] = *LDSP(const bf16x8, p1);                                               \
    SBAR();                                                                                 \
    MFMA16_ACC(acc[1][1], wf[buf][1], af[buf][1]);                                          \
    wf[buf ^ 1][1] = *LDSP(const bf16x8, pw + 1024);                                        \
    as_ = as_ == 4 * 8192 ? 0 : as_ + 8192;                                                 \
    SBAR();                                                                                 \
    MFMA16_ACC(acc[0][2], wf[buf][2], af[buf][0]);                                          \
    wf[buf ^ 1][2] = *LDSP(const bf16x8, pw + 2048);                                        \
    p0 = a0 + as_; PIN(p0);                                                                 \
    SBAR();                                                                                 \
    MFMA16_ACC(acc[1][2], wf[buf][2], af[buf][1]);                                          \
    wf[buf ^ 1][3] = *LDSP(const bf16x8, pw + 3072);                                        \
    p1 = a1 + as_; PIN(p1);                                                                 \
    SBAR();                                                                                 \
    MFMA16_ACC(acc[0][3], wf[buf][3], af[buf][0]);                                          \
    wf[buf ^ 1][4] = *LDSP(const bf16x8, pw + 4096);                                        \
    SBAR();                                                                                 \
    MFMA16_ACC(acc[1][3], wf[buf][3], af[buf][1]);                                          \
    ws_ = ws_ == 11 * 10240 ? 0 : ws_ + 10240;                                              \
    PINS(ws_);                                                                              \
    SBAR();                                                                                 \
    MFMA16_ACC(acc[0][4], wf[buf][4], af[buf][0]);                                          \
    pw = wb + ws_; PIN(pw);                                                                 \
    SBAR();                                                                                 \
    MFMA16_ACC(acc[1][4], wf[buf][4], af[buf][1]);                                          \
    SBAR();                                                                                 \
  } while (0)
    BARRIER();
    STAMP(10);
    af[1][0] = *LDSP(const bf16x8, a0); SBAR();
    af[1][1] = *LDSP(const bf16x8, a1); SBAR();
#pragma unroll
    for (int i = 0; i < 3; ++i) {
      MFMA16_ACC(acc[0][0], wf[3 - i][0], af[(i & 1) ^ 1][0]);
      MFMA16_ACC(acc[1][0], wf[3 - i][0], af[(i & 1) ^ 1][1]);
      asm volatile("s_barrier" ::: "memory");
      af[i & 1][0] = *LDSP(const bf16x8, a0 + (i + 1) * 8192);
      af[i & 1][1] = *LDSP(const bf16x8, a1 + (i + 1) * 8192);
      SBAR();
#pragma unroll
      for (int n = 1; n < 5; ++n) {
        MFMA16_ACC(acc[0][n], wf[3 - i][n], af[(i & 1) ^ 1][0]);
        MFMA16_ACC(acc[1][n], wf[3 - i][n], af[(i & 1) ^ 1][1]);
      }
      SBAR();
    }
    P5_STEP(0);
#pragma unroll 1
    for (int it = 0; it < 11; ++it) {
      P5_STEP(1);
      P5_STEP(0);
    }
#pragma unroll
    for (int n = 0; n < 5; ++n) {
      MFMA16_ACC(acc[0][n], wf[1][n], af[1][0]);
      MFMA16_ACC(acc[1][n], wf[1][n], af[1][1]);
    }
#undef P5_STEP
    MFMA_DONE();
    SBAR();
    STAMP(11);
#pragma unroll
    for (int n = 0; n < 5; ++n) *LDSP(f32x4, lds + L_XCH + (lw * 5 + n) * 1024 + lane * 16) = acc[1][n];
    BARRIER();
    u32x2 o[5];
#pragma unroll
    for (int n = 0; n < 5; ++n) {
      const f32x4 v = acc[0][n] + *LDSP(const f32x4, lds + L_XCH + ((lw ^ 2) * 5 + n) * 1024 + lane * 16);
      o[n] = (u32x2){pk_bf16(v[0] + bf_lo(res[n][0]), v[1] + bf_hi(res[n][0])), pk_bf16(v[2] + bf_lo(res[n][1]), v[3] + bf_hi(res[n][1]))};
    }
    char* const op = out + (size_t)tok * 1280 + cb * 160;
#pragma unroll
    for (int np = 0; np < 2; ++np) *(u32x4*)(op + ((2 * np + (G & 1)) * 4 + (G & 2)) * 8) = widen16(o[2 * np], o[2 * np + 1]);
    *(u32x2*)(op + (16 + G) * 8) = o[4];
    STAMP(12);
  }
  STAMP_OUT(ws, 1);
}

extern "C" __global__ __launch_bounds__(512, 2) void block_fwd(const char* __restrict__ x, const char* __restrict__ wqkv, const char* __restrict__ wo,
                                                               const char* __restrict__ wg, const char* __restrict__ wu,
                                                               const char* __restrict__ wd, char* ws, char* __restrict__ out) {
  __shared__ __attribute__((aligned(1024))) char smem[163840];
  const int lane = threadIdx.x & 63, wave = __builtin_amdgcn_readfirstlane(threadIdx.x >> 6);
  if (wave >= 4) role_b((lchar*)smem, wave - 4, lane, x, wqkv, wo, wg, wu, wd, ws, out);
  else role_a((lchar*)smem, wave, lane, x, ws);
}
extern "C" __global__ void block_end() {}

extern "C" void kernel_launch(void* const* d_in, const int* in_sizes, int n_in, void* d_out, int out_size, void* d_ws, size_t ws_size,
                              hipStream_t stream) {
  block_fwd<<<256, 512, 0, stream>>>((const char*)d_in[0], (const char*)d_in[1], (const char*)d_in[2], (const char*)d_in[4],
                                     (const char*)d_in[5], (const char*)d_in[6], (char*)d_ws, (char*)d_out);
}
```

```cpp
#include <hip/hip_runtime.h>
#include <hip/hip_bf16.h>
#include <stdint.h>

typedef __attribute__((ext_vector_type(8))) short bf16x8;
typedef __attribute__((ext_vector_type(4))) short bf16x4;
typedef __attribute__((ext_vector_type(32))) float f32x32;
typedef __attribute__((ext_vector_type(16))) float f32x16;
typedef __attribute__((ext_vector_type(4))) float f32x4;
typedef __attribute__((ext_vector_type(2))) float f32x2;
typedef __attribute__((ext_vector_type(4))) unsigned u32x4;
typedef __attribute__((ext_vector_type(2))) unsigned u32x2;
typedef unsigned long long u64;
typedef __attribute__((address_space(3))) char lchar;
#define LDSP(T, p) ((__attribute__((address_space(3))) T*)(p))

#define FORCE_SLOW 0
constexpr int DM = 640, FF = 1728;
constexpr float LN_EPS = 1e-5f;
constexpr float QSCALE = 0.22360679774997896f * 1.4426950408889634f;

constexpr int QP_B = 2560, KVP_B = 5376;
constexpr int WS_Q = 0;
constexpr int WS_KV = WS_Q + 32 * 32 * QP_B;
constexpr int WS_O = WS_KV + 32 * 32 * KVP_B;
constexpr int WS_H = WS_O + 32 * 32 * QP_B;
constexpr int WS_A = WS_H + 128 * 20 * 1024;
constexpr int WS_WO = WS_A + 128 * 54 * 1024;
constexpr int WS_WGU = WS_WO + 8 * 20 * 5 * 1024;
constexpr int WS_WD = WS_WGU + 16 * 20 * 14 * 1024;
constexpr int WS_CSP = WS_WD + 8 * 54 * 5 * 1024;
constexpr int WS_ST = WS_CSP + 20 * 3584 * 4;
constexpr int WS_CNT = WS_ST + 8 * 2048 * 8;
constexpr int WS_RS = WS_CNT + 256 * 14 * 128;
constexpr int WS_FS = WS_RS + 2048 * 8;
constexpr int WS_SALT = WS_FS + 256 * 8;
constexpr int WS_END = WS_SALT + 128;

#define MFMA32(a, b, c) __builtin_amdgcn_mfma_f32_32x32x16_bf16(a, b, c, 0, 0, 0)
#define MFMA16(a, b, c) __builtin_amdgcn_mfma_f32_16x16x32_bf16(a, b, c, 0, 0, 0)
#define MFMA2B(a, b, c) __builtin_amdgcn_mfma_f32_32x32x4bf16_1k(a, b, c, 0, 0, 0)
#define MFMA16_ACC(acc, a, b) asm volatile("v_mfma_f32_16x16x32_bf16 %0, %1, %2, %0" : "+v"(acc) : "v"(a), "v"(b))
#define MFMA_DONE() asm volatile("s_nop 7\n\ts_nop 1" ::: "memory")
#define PIN(x) asm volatile("" : "+v"(x))
#define PINS(x) asm volatile("" : "+s"(x))
#define SBAR() __builtin_amdgcn_sched_barrier(0)
#define BARRIER() asm volatile("s_waitcnt lgkmcnt(0)\n\ts_barrier" ::: "memory")
#define WAITV_BARRIER(N) asm volatile("s_waitcnt vmcnt(%0) lgkmcnt(0)\n\ts_barrier" ::"i"(N) : "memory")
#define DRAIN() asm volatile("s_waitcnt vmcnt(0)" ::: "memory")
#define BC8(x) __builtin_bit_cast(bf16x8, x)
constexpr int SC1 = 16;

__device__ __forceinline__ void dma16(const void* sbase, unsigned voff, unsigned lds) {
  asm volatile("s_mov_b32 m0, %2\n\ts_nop 0\n\tglobal_load_lds_dwordx4 %0, %1" ::"v"(voff), "s"(sbase), "s"(lds) : "memory");
}
template <int N>
__device__ __forceinline__ void dma_run(const void* sbase, unsigned voff, unsigned lds) {
#define GL(o) "\n\tglobal_load_lds_dwordx4 %0, %1 offset:" #o " sc1"
  if constexpr (N == 1) asm volatile("s_mov_b32 m0, %2\n\ts_nop 0" GL(0)::"v"(voff), "s"(sbase), "s"(lds) : "memory");
  else if constexpr (N == 2) asm volatile("s_mov_b32 m0, %2\n\ts_nop 0" GL(0) GL(1024)::"v"(voff), "s"(sbase), "s"(lds) : "memory");
  else if constexpr (N == 3) asm volatile("s_mov_b32 m0, %2\n\ts_nop 0" GL(0) GL(1024) GL(2048)::"v"(voff), "s"(sbase), "s"(lds) : "memory");
  else asm volatile("s_mov_b32 m0, %2\n\ts_nop 0" GL(0) GL(1024) GL(2048) GL(3072)::"v"(voff), "s"(sbase), "s"(lds) : "memory");
#undef GL
}
template <int N>
__device__ __forceinline__ void dma_lin(const char* sbase, unsigned voff, unsigned lds) {
#pragma unroll
  for (int i = 0; i + 4 <= N; i += 4) dma_run<4>(sbase + i * 1024, voff, lds + i * 1024);
  if constexpr (N % 4 != 0) dma_run<N % 4>(sbase + (N / 4 * 4) * 1024, voff, lds + (N / 4 * 4) * 1024);
}
__device__ __forceinline__ void touch64(const void* sbase, unsigned voff, unsigned lds) {
  asm volatile("s_mov_b32 m0, %2\n\ts_nop 0\n\tglobal_load_lds_dword %0, %1 sc1" ::"v"(voff), "s"(sbase), "s"(lds) : "memory");
}
__device__ __forceinline__ unsigned pk_bf16(float lo, float hi) {
  typedef __attribute__((ext_vector_type(2))) __bf16 bf2;
  bf2 r = __builtin_convertvector((f32x2){lo, hi}, bf2);
  return __builtin_bit_cast(unsigned, r);
}
__device__ __forceinline__ float dot2_bf16(unsigned a, unsigned b, float c) {
  typedef __attribute__((ext_vector_type(2))) __bf16 bf2;
  return __builtin_amdgcn_fdot2_f32_bf16(__builtin_bit_cast(bf2, a), __builtin_bit_cast(bf2, b), c, false);
}
__device__ __forceinline__ float bf_lo(unsigned w) { return __uint_as_float(w << 16); }
__device__ __forceinline__ float bf_hi(unsigned w) { return __uint_as_float(w & 0xffff0000u); }
__device__ __forceinline__ bf16x4 tr_read(const lchar* p) { return __builtin_amdgcn_ds_read_tr16_b64_v4i16(LDSP(bf16x4, p)); }
__device__ __forceinline__ bf16x8 cat8(bf16x4 a, bf16x4 b) { return __builtin_shufflevector(a, b, 0, 1, 2, 3, 4, 5, 6, 7); }
__device__ __forceinline__ float swap32_add(float v) {
  auto r = __builtin_amdgcn_permlane32_swap(__float_as_uint(v), __float_as_uint(v), false, false);
  return __uint_as_float(r[0]) + __uint_as_float(r[1]);
}
__device__ __forceinline__ float swap16_add(float v) {
  auto r = __builtin_amdgcn_permlane16_swap(__float_as_uint(v), __float_as_uint(v), false, false);
  return __uint_as_float(r[0]) + __uint_as_float(r[1]);
}
__device__ __forceinline__ u32x4 widen32(u32x2 a, u32x2 b) {
  auto r0 = __builtin_amdgcn_permlane32_swap(a[0], b[0], false, false);
  auto r1 = __builtin_amdgcn_permlane32_swap(a[1], b[1], false, false);
  return (u32x4){r0[0], r1[0], r0[1], r1[1]};
}
__device__ __forceinline__ u32x4 widen16(u32x2 a, u32x2 b) {
  auto r0 = __builtin_amdgcn_permlane16_swap(a[0], b[0], false, false);
  auto r1 = __builtin_amdgcn_permlane16_swap(a[1], b[1], false, false);
  return (u32x4){r0[0], r1[0], r0[1], r1[1]};
}
template <typename R>
__device__ __forceinline__ void st128(u32x4 v, R rsrc, unsigned voff, int soff) {
  __builtin_amdgcn_raw_buffer_store_b128(v, rsrc, voff + soff, 0, SC1);
}
constexpr int C_O = 0, C_H = 1, C_A = 2, C_Q = 3, C_W = 4, C_KV = 5, C_A2 = 13, C_N = 14;
__device__ __forceinline__ unsigned* counter(char* ws, int blk, int i) { return (unsigned*)(ws + WS_CNT) + (blk * C_N + i) * 32; }
__device__ __forceinline__ void signal_add(unsigned* c) { __hip_atomic_fetch_add((__attribute__((address_space(1))) unsigned*)c, 1u, __ATOMIC_RELAXED, __HIP_MEMORY_SCOPE_AGENT); }
__device__ __forceinline__ u64 load_tag(const char* ws) {
  u64 t;
  asm volatile("s_load_dwordx2 %0, %1, 0x0\n\ts_waitcnt lgkmcnt(0)" : "=s"(t) : "s"(ws + WS_SALT) : "memory");
  return t + 1;
}
__device__ __forceinline__ u64 flag_load(const char* p) { return __hip_atomic_load((const u64*)p, __ATOMIC_RELAXED, __HIP_MEMORY_SCOPE_AGENT); }
__device__ __forceinline__ void flag_store(char* p, u64 v) { __hip_atomic_store((u64*)p, v, __ATOMIC_RELAXED, __HIP_MEMORY_SCOPE_AGENT); }
__device__ __forceinline__ void wait_started(const char* ws, u64 tag, int lane) {
#pragma unroll 1
  for (unsigned spins = 0; spins < (1u << 18); ++spins) {
    bool ok = true;
#pragma unroll
    for (int i = 0; i < 4; ++i) ok &= flag_load(ws + WS_FS + (i * 64 + lane) * 8) == tag;
    if (__all(ok)) break;
    __builtin_amdgcn_s_sleep(4);
  }
  asm volatile("" ::: "memory");
}
__device__ __forceinline__ void wait_count(unsigned* c, unsigned want, int lane, int n = 1, int stride = 0) {
#pragma unroll 1
  for (unsigned spins = 0; spins < (1u << 18); ++spins) {
    unsigned v = want;
    if (lane < n) asm volatile("global_atomic_add %0, %1, %2, %3 sc0\n\ts_waitcnt vmcnt(0)" : "=&v"(v) : "v"(lane * stride), "v"(0), "s"(c) : "memory");
    if (__all(v >= want)) break;
    __builtin_amdgcn_s_sleep(1);
  }
  asm volatile("" ::: "memory");
}

struct Looks {
  unsigned r0 = 0, r1 = 0, r2 = 0;
  __device__ __forceinline__ void wait(unsigned* c, unsigned want) {
    unsigned t, spins = 1u << 17;
#define LOOK(r) "global_atomic_add " r ", %5, %5, %6 sc0\n\ts_sleep 2\n\t"
#define SEEN(r) "s_waitcnt vmcnt(2)\n\tv_readfirstlane_b32 %3, " r "\n\ts_cmp_ge_u32 %3, %7\n\ts_cbranch_scc1 2f\n\t"
    asm volatile("s_mov_b64 exec, 1\n\t" LOOK("%0") LOOK("%1") "1:\n\t" LOOK("%2") SEEN("%0") LOOK("%0") SEEN("%1") LOOK("%1") SEEN("%2")
                 "s_sub_u32 %4, %4, 1\n\ts_cmp_lg_u32 %4, 0\n\ts_cbranch_scc1 1b\n2:\n\ts_mov_b64 exec, -1"
                 : "+&v"(r0), "+&v"(r1), "+&v"(r2), "=&s"(t), "+s"(spins) : "v"(0), "s"(c), "s"(want) : "memory", "scc");
#undef LOOK
#undef SEEN
  }
  __device__ __forceinline__ void done() { asm volatile("" : "+v"(r0), "+v"(r1), "+v"(r2)::"memory"); }
};
__device__ __forceinline__ void lds_raise(lchar* w) { *LDSP(volatile unsigned, w) = 1; }
__device__ __forceinline__ void lds_await(lchar* w) {
#pragma unroll 1
  for (unsigned spins = 0; spins < (1u << 22); ++spins)
    if (__builtin_amdgcn_readfirstlane(*LDSP(volatile unsigned, w)) != 0) break;
  asm volatile("" ::: "memory");
}
__device__ __forceinline__ void wait_shared(unsigned* c, unsigned want) {
#pragma unroll 1
  for (unsigned spins = 0; spins < (1u << 18); ++spins) {
    if (__all(__hip_atomic_load(c, __ATOMIC_RELAXED, __HIP_MEMORY_SCOPE_AGENT) >= want)) break;
    __builtin_amdgcn_s_sleep(4);
  }
  asm volatile("" ::: "memory");
}

constexpr int L_X1 = 0, L_W1 = 3 * 16384;
constexpr int L_RS = 6 * 16384;
template <bool ISV, typename R, typename F>
__device__ __forceinline__ void qkv_tile(lchar* lds, int wave, int lane, int rt, int ct, R rsw, F&& stats) {
  const int wr = wave >> 1, wc = wave & 1, l15 = lane & 15, G = lane >> 4, hi = lane >> 5;
  const lchar* xa[2];
#pragma unroll
  for (int ks = 0; ks < 2; ++ks) { xa[ks] = lds + L_X1 + (wr * 64 + l15) * 128 + 16 * ((4 * ks + G) ^ (l15 >> 1)); PIN(xa[ks]); }
  const lchar* wa[4][2];
  {
    const int q = l15 >> 2, p = lane & 3;
#pragma unroll
    for (int n = 0; n < 4; ++n)
#pragma unroll
      for (int t = 0; t < 2; ++t) {
        wa[n][t] = lds + L_W1 + (8 * G + 4 * t + q) * 256 + 16 * ((2 * (4 * wc + (n ^ (2 * wr))) + (p >> 1)) ^ ((q << 2) | ((2 * G + t) & 3))) + 8 * (p & 1);
        PIN(wa[n][t]);
      }
  }
  const int sec = ct / 5, cbase = (ct - sec * 5) * 128 + wc * 64;
  unsigned so[4];
  u64 swp[4];
  if (!ISV) {
    const int pstride = sec == 0 ? QP_B : KVP_B, base = (sec == 0 ? WS_Q : WS_KV) + (rt * 2 + wr) * pstride;
#pragma unroll
    for (int n = 0; n < 4; ++n) {
      const int P = ((cbase + (n ^ (2 * wr)) * 16) >> 3) + hi, hp = (P * 205) >> 10, pm = P - hp * 5;
      so[n] = base + l15 * 16 + (pm == 2 ? (hp * 2 + (G & 1)) * 32 * pstride + 2048
                                         : (hp * 2 + (pm > 2)) * 32 * pstride + (G & 1) * 1024 + ((pm == 1 || pm == 4) ? 512 : 0));
      swp[n] = __ballot(pm != 2);
      PIN(so[n]); PINS(swp[n]);
    }
  } else {
#pragma unroll
    for (int n = 0; n < 4; ++n) {
      const int cc = cbase + (n ^ (2 * wr)) * 16 + l15, head = (cc * 3277) >> 16, d = cc - head * 20;
      so[n] = WS_KV + (head * 32 + rt * 2 + wr) * KVP_B + 2560 + G * 320 + d * 16;
      PIN(so[n]);
    }
  }
  f32x4 acc[4][4] = {}, cs[2] = {};
  bf16x8 ones = {0x3f80, 0x3f80, 0x3f80, 0x3f80, 0x3f80, 0x3f80, 0x3f80, 0x3f80};
#pragma unroll
  for (int n = 0; n < 4; ++n)
#pragma unroll
    for (int m = 0; m < 4; ++m) PIN(acc[n][m]);
  PIN(cs[0]); PIN(cs[1]); PIN(ones);
  bf16x8 xf[2][4], wf[4];
#define P1_MM(buf, n, m) do { SBAR(); if (ISV) MFMA16_ACC(acc[n][m], xf[buf][m], wf[n]); else MFMA16_ACC(acc[n][m], wf[n], xf[buf][m]); SBAR(); } while (0)
#define P1_CS(n) do { SBAR(); MFMA16_ACC(cs[n], ones, wf[n]); SBAR(); } while (0)
#define P1_RX(buf, sl, ks, m) xf[buf][m] = *LDSP(const bf16x8, xa[ks] + (sl) * 16384 + (m) * 2048)
#define P1_RW(sl, ks, n) wf[n] = cat8(tr_read(wa[n][0] + (sl) * 16384 + (ks) * 8192), tr_read(wa[n][1] + (sl) * 16384 + (ks) * 8192))
#define P1_H1(sl)                                                                                   \
  _Pragma("unroll") for (int n = 0; n < 4; ++n) {                                                   \
    P1_MM(0, n, 0); if (n < 2) P1_RX(1, sl, 1, 2 * n); P1_MM(0, n, 1);                              \
    if (n < 2) P1_RX(1, sl, 1, 2 * n + 1);                                                          \
    P1_MM(0, n, 2); P1_MM(0, n, 3);                                                                 \
    if (n < 2) P1_CS(n);                                                                            \
    P1_RW(sl, 1, n);                                                                                \
  }
#define P1_H2(sl)                                                                                   \
  do {                                                                                              \
    P1_MM(1, 0, 0); P1_MM(1, 0, 1); P1_MM(1, 0, 2); P1_MM(1, 0, 3); P1_CS(0);                       \
    P1_MM(1, 1, 0); P1_MM(1, 1, 1);                                                                 \
    if ((sl) == 1) WAITV_BARRIER(0); else BARRIER();             \
    P1_RW(sl, 0, 0); P1_MM(1, 1, 2); P1_RX(0, sl, 0, 0); P1_MM(1, 1, 3); P1_RX(0, sl, 0, 1);        \
    P1_CS(1); P1_RW(sl, 0, 1);                                                                      \
    P1_MM(1, 2, 0); P1_RX(0, sl, 0, 2); P1_MM(1, 2, 1); P1_RX(0, sl, 0, 3); P1_MM(1, 2, 2); P1_MM(1, 2, 3); \
    P1_RW(sl, 0, 2);                                                                                \
    P1_MM(1, 3, 0); P1_MM(1, 3, 1); P1_MM(1, 3, 2); P1_MM(1, 3, 3);                                 \
    P1_RW(sl, 0, 3);                                                                                \
  } while (0)
  SBAR();
  stats();
  BARRIER();
#pragma unroll
  for (int i = 0; i < 4; ++i) { P1_RW(0, 0, i); P1_RX(0, 0, 0, i); }
  __builtin_amdgcn_s_waitcnt(0xC07F);
#pragma unroll 1
  for (int s = 0; s < 3; ++s) {
    P1_H1(0); P1_H2(1);
    P1_H1(1); P1_H2(2);
    P1_H1(2); P1_H2(0);
  }
  P1_H1(0);
  P1_CS(0); P1_CS(1);
#pragma unroll
  for (int m = 0; m < 4; ++m) P1_MM(1, 0, m);
  if (lane < 16) {
    *LDSP(float, lds + L_RS + 1024 + (wc * 64 + wr * 32 + lane) * 4) = cs[0][0];
    *LDSP(float, lds + L_RS + 1024 + (wc * 64 + wr * 32 + 16 + lane) * 4) = cs[1][0];
  }
#pragma unroll
  for (int m = 0; m < 4; ++m) P1_MM(1, 1, m);
  BARRIER();
  const lchar* const st = lds + L_RS;
  if (!ISV) {
    f32x2 mr[4];
    f32x4 c4[4];
#pragma unroll
    for (int i = 0; i < 4; ++i) {
      mr[i] = *LDSP(const f32x2, st + (wr * 64 + i * 16 + l15) * 8);
      c4[i] = *LDSP(const f32x4, st + 1024 + (wc * 64 + (i ^ (2 * wr)) * 16 + 4 * G) * 4);
    }
#pragma unroll
    for (int n = 2; n < 4; ++n)
#pragma unroll
      for (int m = 0; m < 4; ++m) P1_MM(1, n, m);
    MFMA_DONE();
    SBAR();
    const float qs = sec == 0 ? QSCALE : 1.0f;
#pragma unroll
    for (int m = 0; m < 4; ++m) mr[m][1] *= qs;
#pragma unroll
    for (int n = 0; n < 4; ++n)
#pragma unroll
      for (int sub = 0; sub < 2; ++sub) {
        const f32x4 a = mr[sub][1] * (acc[n][sub] - mr[sub][0] * c4[n]), b = mr[sub + 2][1] * (acc[n][sub + 2] - mr[sub + 2][0] * c4[n]);
        unsigned w0 = pk_bf16(a[0], a[1]), w1 = pk_bf16(a[2], a[3]), w2 = pk_bf16(b[0], b[1]), w3 = pk_bf16(b[2], b[3]);
        asm volatile("s_mov_b64 exec, %4\n\ts_nop 0\n\tv_permlane16_swap_b32 %0, %2\n\tv_permlane16_swap_b32 %1, %3\n\ts_mov_b64 exec, -1"
                     : "+v"(w0), "+v"(w1), "+v"(w2), "+v"(w3) : "s"(swp[n]));
        st128((u32x4){w0, w1, w2, w3}, rsw, so[n] + sub * 256, 0);
      }
  } else {
    f32x4 mr[4][2];
    float c1[4];
#pragma unroll
    for (int i = 0; i < 4; ++i) {
      mr[i][0] = *LDSP(const f32x4, st + (wr * 64 + i * 16 + 4 * G) * 8);
      mr[i][1] = *LDSP(const f32x4, st + (wr * 64 + i * 16 + 4 * G) * 8 + 16);
      c1[i] = *LDSP(const float, st + 1024 + (wc * 64 + (i ^ (2 * wr)) * 16 + l15) * 4);
    }
#pragma unroll
    for (int n = 2; n < 4; ++n)
#pragma unroll
      for (int m = 0; m < 4; ++m) P1_MM(1, n, m);
    MFMA_DONE();
    SBAR();
#pragma unroll
    for (int n = 0; n < 4; ++n)
#pragma unroll
      for (int t = 0; t < 2; ++t) {
        u32x2 w[2];
#pragma unroll
        for (int i = 0; i < 2; ++i) {
          const int m = 2 * t + i;
          const f32x4& v = acc[n][m];
          w[i] = (u32x2){pk_bf16(mr[m][0][1] * (v[0] - mr[m][0][0] * c1[n]), mr[m][0][3] * (v[1] - mr[m][0][2] * c1[n])),
                         pk_bf16(mr[m][1][1] * (v[2] - mr[m][1][0] * c1[n]), mr[m][1][3] * (v[3] - mr[m][1][2] * c1[n]))};
        }
        st128(widen32(w[0], w[1]), rsw, so[n] + t * 1408, 0);
      }
  }
#undef P1_MM
#undef P1_CS
#undef P1_RX
#undef P1_RW
#undef P1_H1
#undef P1_H2
}

constexpr int CHUNK_B = 4 * KVP_B;
constexpr int L_SCR = 3 * CHUNK_B;
constexpr int L_XR = 2 * CHUNK_B;
constexpr int L_Q = L_SCR + 4 * 8192;
constexpr int L_GO = 110592;
constexpr int L_WO = 163840 - 100 * 1024;
constexpr int L_B4 = 0, B4_B = 28 * 1024;
constexpr int L_A4 = 3 * B4_B, A4_B = 16 * 1024;
constexpr int L_CS = L_A4 + 3 * A4_B;
constexpr int L_TK = L_CS + 1024;
__device__ __forceinline__ int p5_tok(int sl) { return sl == 0 ? 0 : sl == 1 ? L_A4 : 16384; }
__device__ __forceinline__ int p5_slot(int lw, int i) {
  return i == 0 ? 143360 + lw * 5120 : i == 1 ? 32768 + lw * 5120 : i == 2 ? 53248 + lw * 5120 : i == 3 ? 102400 + lw * 5120
                                     : lw < 2 ? 73728 + lw * 5120 : 112640 + lw * 5120;
}

__device__ __forceinline__ float max16(const f32x16& s) {
  float a = fmaxf(fmaxf(s[0], s[1]), s[2]), b = fmaxf(fmaxf(s[3], s[4]), s[5]);
  a = fmaxf(fmaxf(a, s[6]), s[7]); b = fmaxf(fmaxf(b, s[8]), s[9]);
  a = fmaxf(fmaxf(a, s[10]), s[11]); b = fmaxf(fmaxf(b, s[12]), s[13]);
  a = fmaxf(fmaxf(a, s[14]), s[15]);
  a = fmaxf(a, b);
  auto r = __builtin_amdgcn_permlane32_swap(__float_as_uint(a), __float_as_uint(a), false, false);
  return fmaxf(__uint_as_float(r[0]), __uint_as_float(r[1]));
}
__device__ __forceinline__ bf16x8 pack8(const f32x16& s, int o) {
  u32x4 w = {pk_bf16(s[o], s[o + 1]), pk_bf16(s[o + 2], s[o + 3]), pk_bf16(s[o + 4], s[o + 5]), pk_bf16(s[o + 6], s[o + 7])};
  return BC8(w);
}
__device__ __forceinline__ f32x16 lo16(const f32x32& a) { return __builtin_shufflevector(a, a, 0, 1, 2, 3, 4, 5, 6, 7, 8, 9, 10, 11, 12, 13, 14, 15); }
__device__ __forceinline__ f32x16 hi16(const f32x32& a) { return __builtin_shufflevector(a, a, 16, 17, 18, 19, 20, 21, 22, 23, 24, 25, 26, 27, 28, 29, 30, 31); }
__device__ __forceinline__ bf16x4 lo4(bf16x8 a) { return __builtin_shufflevector(a, a, 0, 1, 2, 3); }
__device__ __forceinline__ bf16x4 hi4(bf16x8 a) { return __builtin_shufflevector(a, a, 4, 5, 6, 7); }

__device__ __forceinline__ int v_off(int lane, int half) {
  const int d = lane & 31, e = 2 * half + (lane >> 5);
  const int pick = d >= 28 ? (e == 2 ? 2 : 0) : (e == 2 ? 0 : 2);
  return 2560 + (d < 20 ? e * 320 + d * 16 : 1280 + pick * 32 + (d > 20) * 16);
}
template <typename R>
__device__ __forceinline__ void attn_slow(R rsw, int kv, bf16x8 q1a, bf16x8 q1b, bf16x4 q2, int lane, f32x16& oa, f32x16& ob) {
  const int l31 = lane & 31;
  oa = (f32x16){};
  ob = (f32x16){};
  float ma = -INFINITY, mb = -INFINITY;
#pragma unroll 1
  for (int t = 0; t < 64; ++t) {
    const int p = kv + (t >> 1) * KVP_B;
    const bf16x8 k1 = BC8(__builtin_amdgcn_raw_buffer_load_b128(rsw, lane * 16, p + (t & 1) * 1024, SC1));
    const bf16x4 k2 = __builtin_bit_cast(bf16x4, __builtin_amdgcn_raw_buffer_load_b64(rsw, l31 * 16, p + 2048 + (t & 1) * 8, SC1));
    const bf16x8 v1 = BC8(__builtin_amdgcn_raw_buffer_load_b128(rsw, v_off(lane, 0), p + (t & 1) * 1408, SC1));
    const bf16x8 v2 = BC8(__builtin_amdgcn_raw_buffer_load_b128(rsw, v_off(lane, 1), p + (t & 1) * 1408, SC1));
    const f32x32 s2 = MFMA2B(k2, q2, ((f32x32){}));
    f32x16 sa = MFMA32(k1, q1a, lo16(s2)), sb = MFMA32(k1, q1b, hi16(s2));
    const float na = fmaxf(ma, max16(sa)), nb = fmaxf(mb, max16(sb));
    const float fa = __builtin_amdgcn_exp2f(ma - na), fb = __builtin_amdgcn_exp2f(mb - nb);
    ma = na;
    mb = nb;
#pragma unroll
    for (int r = 0; r < 16; ++r) {
      sa[r] = __builtin_amdgcn_exp2f(sa[r] - na); oa[r] *= fa;
      sb[r] = __builtin_amdgcn_exp2f(sb[r] - nb); ob[r] *= fb;
    }
    oa = MFMA32(v1, pack8(sa, 0), oa);
    oa = MFMA32(v2, pack8(sa, 8), oa);
    ob = MFMA32(v1, pack8(sb, 0), ob);
    ob = MFMA32(v2, pack8(sb, 8), ob);
  }
}
__device__ __forceinline__ float row_sum(const f32x16& o) {
  auto r = __builtin_amdgcn_permlane32_swap(__float_as_uint(o[8]), __float_as_uint(o[8]), false, false);
  return __uint_as_float(r[1]);
}
__device__ __forceinline__ bool unsafe_rows(const f32x16& o) {
  float chk = 0.f;
#pragma unroll
  for (int i = 0; i < 12; ++i) chk += fabsf(o[i]);
  return !(chk < 0x1p100f);
}
__device__ __forceinline__ int blk64(int rt, int cb) { return (rt >> 2) | ((rt & 3) << 3) | (cb << 5); }
__device__ __forceinline__ int blk128(int rt, int cb) { return ((rt >> 3) << 2) | (cb >> 2) | ((rt & 7) << 3) | ((cb & 3) << 6); }
__device__ __forceinline__ int p3_ks(int i, int cb) { return i < 16 ? ((i + 2 * cb) & 15) : 16 + ((i + cb) & 3); }
__device__ __forceinline__ int p5_grp(int i, int cb) {
  const int r = i + (i < 7 ? cb * 7 >> 3 : cb * 6 >> 3);
  return i >= 13 ? 13 : i < 7 ? (r >= 7 ? r - 7 : r) : (r >= 13 ? r - 6 : r);
}
__device__ __forceinline__ unsigned unit_off(int U) { return (U >> 3) * 1024 + ((U >> 1) & 3) * 256 + (U & 1) * 8; }

__device__ __forceinline__ int stat_row(int wave, int lane) {
  return ((blockIdx.x & 7) >> 1) * 512 + ((blockIdx.x >> 3) * 2 + (blockIdx.x & 1)) * 8 + wave * 2 + (lane >> 5);
}
struct QkvLoader {
  const char *xb, *wb;
  unsigned xv, wv, slot;
  int ks;
  __device__ __forceinline__ QkvLoader(const char* x, const char* wqkv, int rt, int ct, int lw, int lane) {
    xv = (lane >> 3) * 1280 + 16 * ((lane & 7) ^ ((4 * lw + (lane >> 4)) & 7));
    wv = (lane >> 4) * 3840 + 16 * ((lane & 15) ^ (((lane >> 4) << 2) | lw));
    xb = x + (size_t)(rt * 128 + lw * 8) * 1280;
    wb = wqkv + (size_t)(lw * 4) * 3840 + ct * 256;
    ks = (ct + (rt * 5 >> 1)) % 10;
    slot = lw * 1024;
  }
  __device__ __forceinline__ void issue(unsigned lbase) {
#pragma unroll
    for (int i = 0; i < 4; ++i) {
      dma16(xb + i * 32 * 1280 + ks * 128, xv, lbase + L_X1 + slot + i * 4096);
      dma16(wb + (size_t)(ks * 64 + i * 16) * 3840, wv, lbase + L_W1 + slot + i * 4096);
    }
    ks = ks == 9 ? 0 : ks + 1;
    slot = slot >= 32768 ? slot - 32768 : slot + 16384;
  }
};

__device__ __forceinline__ void role_a(lchar* lds, int wave, int lane, const u32x2 (&v)[5], int rt, int ct, char* ws) {
  const auto rsw = __builtin_amdgcn_make_buffer_rsrc(ws, 0, WS_END, 0x00020000);
  const int l31 = lane & 31, hi = lane >> 5;
  {
    const int row = stat_row(wave, lane);
    if (wave == 0 && lane < C_N) __hip_atomic_store(counter(ws, blockIdx.x, lane), 0u, __ATOMIC_RELAXED, __HIP_MEMORY_SCOPE_AGENT);
    if (wave == 1) {
      const unsigned fill = (lane & 1) ? 0u : 0x3f803f80u;
      st128((u32x4){fill, fill, fill, fill}, rsw, (blockIdx.x * 4 + (lane >> 4)) * KVP_B + ((lane >> 3) & 1) * 1408 + (lane & 7) * 16, WS_KV + 2560 + 20 * 64);
    }
    auto stats = [&]() {
      auto sum32 = [](float t) {
        t += __uint_as_float(__builtin_amdgcn_update_dpp(0, __float_as_uint(t), 0xB1, 0xf, 0xf, true));
        t += __uint_as_float(__builtin_amdgcn_update_dpp(0, __float_as_uint(t), 0x4E, 0xf, 0xf, true));
        t += __uint_as_float(__builtin_amdgcn_update_dpp(0, __float_as_uint(t), 0x141, 0xf, 0xf, true));
        t += __uint_as_float(__builtin_amdgcn_update_dpp(0, __float_as_uint(t), 0x140, 0xf, 0xf, true));
        return swap16_add(t);
      };
      float t = 0.f;
#pragma unroll
      for (int i = 0; i < 5; ++i) t += (bf_lo(v[i][0]) + bf_hi(v[i][0])) + (bf_lo(v[i][1]) + bf_hi(v[i][1]));
      const float mu = sum32(t) * (1.0f / DM);
      t = 0.f;
#pragma unroll
      for (int i = 0; i < 5; ++i) {
        const float a = bf_lo(v[i][0]) - mu, b = bf_hi(v[i][0]) - mu, c = bf_lo(v[i][1]) - mu, d = bf_hi(v[i][1]) - mu;
        t += (a * a + b * b) + (c * c + d * d);
      }
      const float rstd = __builtin_amdgcn_rsqf(sum32(t) * (1.0f / DM) + LN_EPS);
      if (l31 == 0) __builtin_amdgcn_raw_buffer_store_b64((u32x2){__float_as_uint(mu), __float_as_uint(rstd)}, rsw, row * 8, WS_RS, SC1);
    };
    if (ct >= 15) {
      stats();
      WAITV_BARRIER(0);
    } else {
      if (ct >= 10) qkv_tile<true>(lds, wave, lane, rt, ct, rsw, stats);
      else qkv_tile<false>(lds, wave, lane, rt, ct, rsw, stats);
      if (ct >= 5 && rt >= 2) BARRIER();
      else WAITV_BARRIER(0);
    }
  }
  const int rt3 = (blockIdx.x & 7) * 4 + ((blockIdx.x >> 3) & 3), cb3 = blockIdx.x >> 5, tok3 = (rt3 * 4 + wave) * 16 + (lane & 15);
  u32x2 res[5];
  int so[20];
  unsigned vm, vr, vr2, ho[3];
  unsigned* ocnt = counter(ws, blk64(((blockIdx.x >> 3) & 7) * 4 + wave, lane & 7), C_O);
  {
    const int G = lane >> 4;
#pragma unroll
    for (int i = 0; i < 20; ++i) {
      so[i] = WS_O + ((i < 16 ? 2 * p3_ks(i, cb3) : 8 * (p3_ks(i, cb3) - 16)) * 32 + rt3) * QP_B;
      PINS(so[i]);
    }
    vm = (G >> 1) * 32 * QP_B + (wave >> 1) * 1024 + (G & 1) * 512 + ((wave & 1) * 16 + (lane & 15)) * 16;
    vr = 2 * G * 32 * QP_B + 2048 + (wave * 16 + (lane & 15)) * 8;
    vr2 = vr + 32 * QP_B;
#pragma unroll
    for (int i = 0; i < 3; ++i) {
      ho[i] = WS_H + (rt3 * 4 + wave) * 20480 + (lane & 15) * 16 + unit_off(cb3 * 20 + (i < 2 ? (2 * i + (G & 1)) * 4 + (G & 2) : 16 + G));
      PIN(ho[i]);
    }
    PIN(vm); PIN(vr); PIN(vr2); PIN(ocnt);
  }
  {
    const int head = (blockIdx.x & 7) * 4 + (blockIdx.x >> 6), pair = ((blockIdx.x >> 3) & 7) * 4 + wave;
    const lchar* ka = lds + lane * 16;
    const lchar* kb = lds + 2048 + l31 * 16;
    const lchar* va[2] = {lds + v_off(lane, 0), lds + v_off(lane, 1)};
    unsigned oo = WS_O + (head * 32 + pair) * QP_B + hi * 1024 + l31 * 16, oo2 = WS_O + (head * 32 + pair) * QP_B + 2048 + lane * 8;
    PIN(oo); PIN(oo2);
#define KOFF(t) (((t) >> 1) * KVP_B + ((t) & 1) * 1024)
#define VRD(t, hf) *LDSP(const bf16x8, va[hf] + ((t) >> 1) * KVP_B + ((t) & 1) * 1408)
    bf16x8 k1, k2, v1, v2 = {0, 0, 0, 0, 0, 0, 0, 0};
    u32x4 pa, pb = {0, 0, 0, 0};
    f32x32 negm;
    f32x16 sa[2], sb[2], oa = {}, ob = {};
    const lchar* qp = lds + L_Q + wave * QP_B + lane * 16;
    const lchar* qr = lds + L_Q + wave * QP_B + 2048 + l31 * 16 + hi * 8;
    PIN(v2); PIN(pb); PIN(oa); PIN(ob); PIN(qp); PIN(qr); PIN(ka); PIN(kb); PIN(va[0]); PIN(va[1]);
    WAITV_BARRIER(0);
    k2 = *LDSP(const bf16x8, kb);
    const bf16x4 q2 = *LDSP(const bf16x4, qr);
    const bf16x8 k0 = *LDSP(const bf16x8, ka);
    const bf16x8 q1a = *LDSP(const bf16x8, qp), q1b = *LDSP(const bf16x8, qp + 1024);
    k1 = *LDSP(const bf16x8, ka + KOFF(1));
    v1 = VRD(0, 0);
    SBAR();
    {
      const f32x32 s2 = MFMA2B(lo4(k2), q2, ((f32x32){}));
      sa[0] = MFMA32(k0, q1a, lo16(s2));
      sb[0] = MFMA32(k0, q1b, hi16(s2));
      const float ma = -max16(sa[0]), mb = -max16(sb[0]);
#pragma unroll
      for (int r = 0; r < 16; ++r) { negm[r] = ma; negm[16 + r] = mb; sa[0][r] += ma; sb[0][r] += mb; }
    }
#define EX(S, r) S[r] = __builtin_amdgcn_exp2f(S[r])
#define G1(S) do { EX(S, 0); EX(S, 1); EX(S, 2); EX(S, 3); EX(S, 4); PIN(S); SBAR(); } while (0)
#define G2(S, P) do { EX(S, 5); SBAR(); P[0] = pk_bf16(S[0], S[1]); SBAR(); EX(S, 6); SBAR(); P[1] = pk_bf16(S[2], S[3]); SBAR(); \
                      EX(S, 7); SBAR(); P[2] = pk_bf16(S[4], S[5]); SBAR(); P[3] = pk_bf16(S[6], S[7]); PIN(P); SBAR(); } while (0)
#define G3(S) do { EX(S, 8); EX(S, 9); EX(S, 10); EX(S, 11); EX(S, 12); PIN(S); SBAR(); } while (0)
#define G4(S, P) do { EX(S, 13); SBAR(); P[0] = pk_bf16(S[8], S[9]); SBAR(); EX(S, 14); SBAR(); P[1] = pk_bf16(S[10], S[11]); SBAR(); \
                      EX(S, 15); SBAR(); P[2] = pk_bf16(S[12], S[13]); SBAR(); P[3] = pk_bf16(S[14], S[15]); PIN(P); SBAR(); } while (0)
    __builtin_amdgcn_s_waitcnt(0xC07F);
    int slot = 0;
#pragma unroll 1
    for (int c = 0; c < 8; ++c) {
      const int adv = (slot == 2) ? -2 * CHUNK_B : CHUNK_B;
      slot = (slot == 2) ? 0 : slot + 1;
#pragma unroll
      for (int j = 0; j < 8; ++j) {
        const int cur = j & 1, nxt = cur ^ 1;
        if (j == 6) {
          asm volatile("s_barrier" ::: "memory");
          ka += adv;
          kb += adv;
        }
        SBAR();
        G1(sa[cur]);
        { const f32x32 t = MFMA2B(cur ? lo4(k2) : hi4(k2), q2, negm); sa[nxt] = lo16(t); sb[nxt] = hi16(t); }
        SBAR();
        if (!cur) { k2 = *LDSP(const bf16x8, kb + (((j + 2) & 7) >> 1) * KVP_B); SBAR(); }
        G2(sa[cur], pa);
        G3(sa[cur]);
        ob = MFMA32(v2, BC8(pb), ob); SBAR();
        v2 = VRD(j, 1); SBAR();
        G4(sa[cur], pb);
        oa = MFMA32(v1, BC8(pa), oa); SBAR();
        G1(sb[cur]);
        sa[nxt] = MFMA32(k1, q1a, sa[nxt]); SBAR();
        G2(sb[cur], pa);
        sb[nxt] = MFMA32(k1, q1b, sb[nxt]); SBAR();
        k1 = *LDSP(const bf16x8, ka + KOFF((j + 2) & 7)); SBAR();
        G3(sb[cur]);
        oa = MFMA32(v2, BC8(pb), oa); SBAR();
        G4(sb[cur], pb);
        ob = MFMA32(v1, BC8(pa), ob); SBAR();
        if (j == 7) { va[0] += adv; va[1] += adv; }
        v1 = VRD((j + 1) & 7, 0); SBAR();
      }
    }
    ob = MFMA32(v2, BC8(pb), ob);
#undef EX
#undef G1
#undef G2
#undef G3
#undef G4
#undef KOFF
#undef VRD
    auto store_o = [&]() {
      const float ra = __builtin_amdgcn_rcpf(row_sum(oa)), rb = __builtin_amdgcn_rcpf(row_sum(ob));
      u32x2 wa[3], wb[3];
#pragma unroll
      for (int g = 0; g < 3; ++g) {
        wa[g] = (u32x2){pk_bf16(oa[4 * g] * ra, oa[4 * g + 1] * ra), pk_bf16(oa[4 * g + 2] * ra, oa[4 * g + 3] * ra)};
        wb[g] = (u32x2){pk_bf16(ob[4 * g] * rb, ob[4 * g + 1] * rb), pk_bf16(ob[4 * g + 2] * rb, ob[4 * g + 3] * rb)};
      }
      st128(widen32(wa[0], wb[0]), rsw, oo, 0);
      st128(widen32(wa[1], wb[1]), rsw, oo + 512, 0);
      const u32x4 r = widen32(wa[2], wb[2]);
      __builtin_amdgcn_raw_buffer_store_b64((u32x2){r[0], r[1]}, rsw, oo2, 0, SC1);
    };
    store_o();
    SBAR();
#pragma unroll
    for (int n = 0; n < 5; ++n) res[n] = *LDSP(const u32x2, lds + L_XR + wave * 3072 + (lane & 15) * 160 + (n * 4 + (lane >> 4)) * 8);
    BARRIER();
    if (__builtin_expect(FORCE_SLOW || __any(unsafe_rows(oa) | unsafe_rows(ob)), 0)) {
      attn_slow(rsw, WS_KV + head * 32 * KVP_B, q1a, q1b, q2, lane, oa, ob);
      DRAIN();
      store_o();
    }
  }
  {
    const lchar* wb0 = lds + L_WO + lane * 16;
    const lchar* wb1 = wb0 + 40960;
    PIN(wb0); PIN(wb1);
    bf16x8 wf[8][5];
#pragma unroll
    for (int i = 0; i < 5; ++i)
#pragma unroll
      for (int n = 0; n < 5; ++n) wf[i][n] = *LDSP(const bf16x8, wb0 + i * 5120 + n * 1024);
    f32x4 acc[5];
#pragma unroll
    for (int n = 0; n < 5; ++n) {
      acc[n] = (f32x4){bf_lo(res[n][0]), bf_hi(res[n][0]), bf_lo(res[n][1]), bf_hi(res[n][1])};
      PIN(acc[n]);
    }
    DRAIN();
    if (lane < 8) signal_add(ocnt);
#pragma unroll
    for (int i = 5; i < 8; ++i)
#pragma unroll
      for (int n = 0; n < 5; ++n) wf[i][n] = *LDSP(const bf16x8, wb0 + i * 5120 + n * 1024);
    __builtin_amdgcn_s_waitcnt(0xC07F);
    BARRIER();
    constexpr int AW = 8;
    u32x4 a[AW];
    auto load_a = [&](int i) -> u32x4 {
      if (i < 16) return __builtin_amdgcn_raw_buffer_load_b128(rsw, vm, so[i], SC1);
      const u32x2 lo = __builtin_amdgcn_raw_buffer_load_b64(rsw, vr, so[i], SC1);
      const u32x2 hi2 = __builtin_amdgcn_raw_buffer_load_b64(rsw, vr2, so[i], SC1);
      return (u32x4){lo[0], lo[1], hi2[0], hi2[1]};
    };
#pragma unroll
    for (int i = 0; i < AW; ++i) { a[i] = load_a(i); SBAR(); }
#pragma unroll
    for (int i = 0; i < 20; ++i) {
#pragma unroll
      for (int n = 0; n < 5; ++n) {
        SBAR();
        MFMA16_ACC(acc[n], wf[i % 8][n], a[i % AW]);
        SBAR();
        const int j = (i - 1) * 4 + n;
        if (i >= 1 && n < 4 && j < 60) wf[(j / 5) % 8][j % 5] = *LDSP(const bf16x8, wb1 + j * 1024);
      }
      SBAR();
      if (i + AW < 20) a[i % AW] = load_a(i + AW);
    }
    MFMA_DONE();
    SBAR();
#pragma unroll
    for (int n = 0; n < 5; ++n) res[n] = (u32x2){pk_bf16(acc[n][0], acc[n][1]), pk_bf16(acc[n][2], acc[n][3])};
    st128(widen16(res[0], res[1]), rsw, ho[0], 0);
    st128(widen16(res[2], res[3]), rsw, ho[1], 0);
    __builtin_amdgcn_raw_buffer_store_b64(res[4], rsw, ho[2], 0, SC1);
    SBAR();
    float s = 0.f, q = 0.f, s2 = 0.f, q2 = 0.f;
#pragma unroll
    for (int n = 0; n < 5; ++n) {
      s = dot2_bf16(res[n][0], 0x3f803f80u, s); q = dot2_bf16(res[n][0], res[n][0], q);
      s2 = dot2_bf16(res[n][1], 0x3f803f80u, s2); q2 = dot2_bf16(res[n][1], res[n][1], q2);
    }
    s += s2; q += q2;
    s = swap32_add(swap16_add(s));
    q = swap32_add(swap16_add(q));
    if (lane < 16) __builtin_amdgcn_raw_buffer_store_b64((u32x2){__float_as_uint(s), __float_as_uint(q)}, rsw, tok3 * 8, WS_ST + cb3 * 16384, SC1);
    SBAR();
  }
  {
    const int rt = ((blockIdx.x & 7) >> 2) * 8 + ((blockIdx.x >> 3) & 7), cb = (blockIdx.x & 3) * 4 + (blockIdx.x >> 6);
    const int wr = wave >> 1, wc = wave & 1, G = lane >> 4;
    f32x4 acc[7][4] = {};
    bf16x8 af[2][4], bf[7];
    const lchar* ab = lds + L_A4 + wr * 8 * 1024 + lane * 16;
    const lchar* bb = lds + L_B4 + wc * 7 * 1024 + lane * 16;
    const lchar* bb2 = bb + 2 * B4_B;
    PIN(ab); PIN(bb); PIN(bb2);
#pragma unroll
    for (int n = 0; n < 7; ++n)
#pragma unroll
      for (int m = 0; m < 4; ++m) PIN(acc[n][m]);
    unsigned ao[5];
    unsigned* acnt = counter(ws, blk64(rt * 2 + wr, lane & 7), C_A);
    {
      const int w = cb * 2 + wc, base = WS_A + (rt * 8 + wr * 4) * 54 * 1024 + (lane & 15) * 16;
      ao[0] = w < 31 ? base + (G & 1) * 54 * 1024 + w * 1024 + hi * 256 : WS_END;
      ao[2] = w < 31 ? base + (G & 1) * 54 * 1024 + 31 * 1024 + (w * 3 + hi) * 256 : WS_END;
      ao[1] = ao[0] + 2 * 54 * 1024;
      ao[3] = ao[2] + 2 * 54 * 1024;
      ao[4] = w < 30 ? base + (2 * (G & 1) + hi) * 54 * 1024 + 31 * 1024 + (w * 3 + 2) * 256 : WS_END;
#pragma unroll
      for (int i = 0; i < 5; ++i) PIN(ao[i]);
      PIN(acnt);
    }
#define K4_B(sl) ((sl) == 2 ? bb2 : bb + (sl) * B4_B)
#pragma unroll
    for (int n = 0; n < 7; ++n) bf[n] = *LDSP(const bf16x8, bb + n * 1024);
    WAITV_BARRIER(0);
    if (wave == 0) {
      Looks lk;
      lk.wait(counter(ws, blockIdx.x, C_H), 16);
      BARRIER();
      DRAIN();
      lk.done();
    } else {
      BARRIER();
    }
    BARRIER();
#pragma unroll
    for (int m = 0; m < 4; ++m) af[0][m] = *LDSP(const bf16x8, ab + 2 * m * 1024);
    f32x4 cs[7], tk[4];
#define K4_MM(buf, n, m) MFMA16_ACC(acc[n][m], bf[n], af[buf][m])
#define K4_H1(sl)                                                                     \
  _Pragma("unroll") for (int n = 0; n < 7; ++n) {                                     \
    SBAR(); K4_MM(0, n, 0); K4_MM(0, n, 1); SBAR();                                   \
    if (n < 4) af[1][n] = *LDSP(const bf16x8, ab + (sl) * A4_B + (2 * n + 1) * 1024); \
    SBAR(); K4_MM(0, n, 2); K4_MM(0, n, 3); SBAR();                                   \
    bf[n] = *LDSP(const bf16x8, K4_B(sl) + (14 + n) * 1024);                          \
  }
#define K4_H2(sl)                                                                     \
  _Pragma("unroll") for (int n = 0; n < 7; ++n) {                                     \
    SBAR(); K4_MM(1, n, 0); K4_MM(1, n, 1); SBAR();                                   \
    if (n >= 2 && n < 6) af[0][n - 2] = *LDSP(const bf16x8, ab + (sl) * A4_B + 2 * (n - 2) * 1024); \
    SBAR(); K4_MM(1, n, 2); K4_MM(1, n, 3); SBAR();                                   \
    if (n == 1) { BARRIER(); bf[0] = *LDSP(const bf16x8, K4_B(sl)); }                 \
    if (n >= 1) bf[n] = *LDSP(const bf16x8, K4_B(sl) + n * 1024);                     \
  }
    __builtin_amdgcn_s_waitcnt(0xC07F);
#pragma unroll 1
    for (int s = 0; s < 3; ++s) {
      K4_H1(0); K4_H2(1);
      K4_H1(1); K4_H2(2);
      K4_H1(2); K4_H2(0);
    }
    bf16x8 bx[3];
#define K4_T(g) (((g) & 1) * 3 + ((g) >> 1))
#define K4_CS(n) cs[n] = *LDSP(const f32x4, lds + L_CS + ((wc * 7 + (n)) * 16 + G * 4) * 4)
#pragma unroll
    for (int g = 0; g < 4; ++g) {
      const int n = K4_T(g);
      SBAR(); K4_MM(0, n, 0); SBAR();
      af[1][g] = *LDSP(const bf16x8, ab + (2 * g + 1) * 1024);
      SBAR(); K4_MM(0, n, 1); SBAR();
      if (g < 3) bx[g] = *LDSP(const bf16x8, bb + (14 + (g == 0 ? 2 : 4 + g)) * 1024);
      SBAR(); K4_MM(0, n, 2); K4_MM(0, n, 3); SBAR();
      bf[n] = *LDSP(const bf16x8, bb + (14 + n) * 1024);
    }
#pragma unroll
    for (int g = 0; g < 4; ++g) {
      const int n = K4_T(g);
      SBAR(); K4_MM(1, n, 0); K4_MM(1, n, 1); SBAR();
      tk[g] = *LDSP(const f32x4, lds + L_TK + (wr * 64 + g * 16 + (lane & 15)) * 16);
      SBAR(); K4_MM(1, n, 2); K4_MM(1, n, 3); SBAR();
      K4_CS(n);
    }
#undef K4_T
    MFMA_DONE();
    SBAR();
#undef K4_B
#undef K4_H1
#undef K4_H2
    auto geglu = [&](const f32x4& g, const f32x4& u, const f32x4& cg, const f32x4& cu, const f32x4& t) -> u32x2 {
      const f32x4 gg = g + t[0] * cg, uu = u + t[0] * cu;
      const f32x4 z = gg * (t[1] + t[2] * (gg * gg));
      f32x4 e, r;
#pragma unroll
      for (int j = 0; j < 4; ++j) e[j] = __builtin_amdgcn_exp2f(z[j]);
      e = t[3] + e * t[3];
#pragma unroll
      for (int j = 0; j < 4; ++j) r[j] = __builtin_amdgcn_rcpf(e[j]);
      const f32x4 o = gg * uu * r;
      return (u32x2){pk_bf16(o[0], o[1]), pk_bf16(o[2], o[3])};
    };
    auto tile_pair = [&](int n, int mp) {
      return widen16(geglu(acc[n][2 * mp], acc[n + 3][2 * mp], cs[n], cs[n + 3], tk[2 * mp]),
                     geglu(acc[n][2 * mp + 1], acc[n + 3][2 * mp + 1], cs[n], cs[n + 3], tk[2 * mp + 1]));
    };
#pragma unroll
    for (int n = 0; n < 2; ++n)
#pragma unroll
      for (int mp = 0; mp < 2; ++mp) st128(tile_pair(n, mp), rsw, ao[mp] + n * 512, 0);
    SBAR();
    asm volatile("s_barrier" ::: "memory");
    K4_CS(2); K4_CS(5); K4_CS(6);
    SBAR();
#pragma unroll
    for (int g = 0; g < 6; ++g) {
      const int n = g % 3 == 0 ? 2 : 4 + g % 3;
#pragma unroll
      for (int m = 0; m < 4; ++m) {
        if (g < 3) K4_MM(0, n, m);
        else MFMA16_ACC(acc[n][m], bx[g - 3], af[1][m]);
      }
    }
#undef K4_MM
#undef K4_CS
    MFMA_DONE();
    SBAR();
    u32x4 l0 = tile_pair(2, 0);
    PIN(l0);
    DRAIN();
    if (lane < 8) signal_add(acnt);
    st128(l0, rsw, ao[2], 0);
    st128(tile_pair(2, 1), rsw, ao[3], 0);
    SBAR();
    {
      f32x4 cg, cu;
#pragma unroll
      for (int j = 0; j < 4; ++j) {
        auto r = __builtin_amdgcn_permlane32_swap(__float_as_uint(cs[6][j]), __float_as_uint(cs[6][j]), false, false);
        cg[j] = __uint_as_float(r[0]);
        cu[j] = __uint_as_float(r[1]);
      }
      u32x2 r2[2];
#pragma unroll
      for (int mp = 0; mp < 2; ++mp) {
        f32x4 g, u;
#pragma unroll
        for (int j = 0; j < 4; ++j) {
          auto r = __builtin_amdgcn_permlane32_swap(__float_as_uint(acc[6][2 * mp][j]), __float_as_uint(acc[6][2 * mp + 1][j]), false, false);
          g[j] = __uint_as_float(r[0]);
          u[j] = __uint_as_float(r[1]);
        }
        f32x4 t = {};
#pragma unroll
        for (int j = 0; j < 4; ++j) t[j] = hi ? tk[2 * mp + 1][j] : tk[2 * mp][j];
        r2[mp] = geglu(g, u, cg, cu, t);
      }
      st128(widen16(r2[0], r2[1]), rsw, ao[4], 0);
    }
    DRAIN();
    if (lane < 8) signal_add(acnt + (C_A2 - C_A) * 32);
  }
  {
    const int rt = (blockIdx.x & 7) * 4 + ((blockIdx.x >> 3) & 3), cb = blockIdx.x >> 5;
    const char* const ap = ws + WS_A + (rt * 4 + wave) * 54 * 1024;
    const unsigned dst = (unsigned)(uintptr_t)lds + wave * 4096;
    BARRIER();
    unsigned* const cnt2 = counter(ws, blockIdx.x, C_A2);
    unsigned seen = 0;
#pragma unroll
    for (int i = 3; i < 14; ++i) {
      const char* src = ap + p5_grp(i, cb) * 4096;
      const int j = (i - 6) >> 1, hf = i & 1;
      const char* wsrc = ws + WS_WD + (cb * 54 + p5_grp(11 + (i >= 6 && i < 12 ? j : 0), cb) * 4 + wave) * 5120 + hf * 3072;
      PINS(src); PINS(wsrc);
      if (i >= 8 && i < 13) WAITV_BARRIER(9); else if (i == 7) WAITV_BARRIER(7); else if (i == 13) WAITV_BARRIER(6); else WAITV_BARRIER(4);
      dma_run<4>(src, lane * 16, dst + p5_tok(i % 3));
      if (i == 4) asm volatile("s_mov_b64 exec, %3\n\tglobal_atomic_add %0, %1, %1, %2 sc0\n\ts_mov_b64 exec, -1" : "+v"(seen) : "v"(0), "s"(cnt2), "s"(1ull) : "memory");
      if (i == 6) {
        PIN(seen);
        if (__builtin_amdgcn_readfirstlane(seen) < 32) {
          DRAIN();
          PIN(seen);
          wait_count(cnt2, 32, lane);
        }
      }
      if (i >= 6 && i < 12) {
        if (hf) dma_run<2>(wsrc, lane * 16, (unsigned)(uintptr_t)lds + p5_slot(wave, j) + 3072);
        else dma_run<3>(wsrc, lane * 16, (unsigned)(uintptr_t)lds + p5_slot(wave, j));
      }
    }
    WAITV_BARRIER(4);
    WAITV_BARRIER(0);
  }
}

__device__ __forceinline__ void role_b(lchar* lds, int lw, int lane, QkvLoader& ld, int rt1, int ct, const char* __restrict__ x, const char* __restrict__ wo,
                                       const char* __restrict__ wg, const char* __restrict__ wu, const char* __restrict__ wd,
                                       char* ws, char* __restrict__ out) {
  const unsigned lbase = (unsigned)(uintptr_t)lds;
  unsigned* late_cnt = nullptr;
  bool late = false;
  {
    u64 tag;
    *LDSP(volatile u64, lds + L_GO) = 0;
    if (ct >= 15) {
      tag = load_tag(ws);
      WAITV_BARRIER(0);
      if (lw == 0 && lane == 0) flag_store(ws + WS_FS + blockIdx.x * 8, tag);
      if (lw == 3) {
        __builtin_amdgcn_s_sleep(100);
        wait_started(ws, tag, lane);
        if (blockIdx.x == 0 && lane == 0) flag_store(ws + WS_SALT, tag);
      }
    } else {
      const int rt = rt1;
      auto issue = [&]() { ld.issue(lbase); };
      issue();
      tag = load_tag(ws);
      WAITV_BARRIER(8);
      issue();
      WAITV_BARRIER(8);
      issue();
      if (lw == 0) { if (lane == 0) flag_store(ws + WS_FS + blockIdx.x * 8, tag); }
      WAITV_BARRIER(8);
#pragma unroll 1
      for (int s = 3; s < 8; ++s) {
        issue();
        if (s == 6 && lw == 3) dma_run<2>(ws + WS_FS, lane * 16, lbase + L_RS + 2048);
        WAITV_BARRIER(8);
      }
      const u32x4 f0 = *LDSP(const u32x4, lds + L_RS + 2048 + lane * 16), f1 = *LDSP(const u32x4, lds + L_RS + 3072 + lane * 16);
      issue();
      if (lw == 3) {
        const unsigned tl = (unsigned)tag, th = (unsigned)(tag >> 32);
        if (__any(((f0[0] ^ tl) | (f0[2] ^ tl) | (f1[0] ^ tl) | (f1[2] ^ tl) | (f0[1] ^ th) | (f0[3] ^ th) | (f1[1] ^ th) | (f1[3] ^ th)) != 0))
          wait_started(ws, tag, lane);
        dma_run<1>(ws + WS_RS + rt * 1024, lane * 16, lbase + L_RS);
      }
      WAITV_BARRIER(8);
      if (lw == 3) WAITV_BARRIER(1); else WAITV_BARRIER(0);
      WAITV_BARRIER(0);
      const int sec = ct / 5, c0 = (ct - sec * 5) * 128, hh = c0 / 20 + (lane >> 3), qb = lane & 7;
      unsigned* cnt = counter(ws, (hh >> 2) | (qb << 3) | ((hh & 3) << 6), sec == 0 ? C_Q : C_KV + (rt >> 1));
      PIN(cnt);
      const bool send = lw == 3 && hh * 20 < c0 + 128 && (sec != 0 || qb == (rt >> 1));
      BARRIER();
      if (sec != 0 && rt >= 2) { late_cnt = cnt; late = send; }
      else if (send) signal_add(cnt);
    }
  }
  const auto rsw = __builtin_amdgcn_make_buffer_rsrc(ws, 0, WS_END, 0x00020000);
  const int rt = (blockIdx.x & 7) * 4 + ((blockIdx.x >> 3) & 3), cb = blockIdx.x >> 5;
  const int rt4 = ((blockIdx.x & 7) >> 2) * 8 + ((blockIdx.x >> 3) & 7), cb4 = (blockIdx.x & 3) * 4 + (blockIdx.x >> 6);
  const int G = lane >> 4;
  int t16 = rt * 4 + lw;
  PINS(t16);
  float csum = 0.f;
  u32x4 wr[6][5];
  auto wd_off = [&](int i) { return WS_WD + (cb * 54 + p5_grp(i, cb) * 4 + lw) * 5120; };
  auto wd_regs = [&](int set, int i) {
    const int o = wd_off(i);
#pragma unroll
    for (int n = 0; n < 5; ++n) wr[set][n] = __builtin_amdgcn_raw_buffer_load_b128(rsw, lane * 16 + (n & 3) * 1024, o + (n >> 2) * 4096, SC1);
  };
  {
    const int head = (blockIdx.x & 7) * 4 + (blockIdx.x >> 6), qblk = (blockIdx.x >> 3) & 7;
    const char* kv = ws + WS_KV + head * 32 * KVP_B + lw * 7168;
    const char* qsrc = ws + WS_Q + (head * 32 + qblk * 4) * QP_B;
    PINS(kv); PINS(qsrc);
    const unsigned sides = (head * 20) / 128 == (head * 20 + 19) / 128 ? 1 : 2;
    int ch = 0, slot = 0;
    auto issue = [&]() {
      if (lw < 3) dma_lin<7>(kv + ch * CHUNK_B, lane * 16, lbase + slot + lw * 7168);
      ++ch;
      slot = (slot == 2 * CHUNK_B) ? 0 : slot + CHUNK_B;
    };
    Looks lk;
    if (lw == 0 || lw == 3) {
      lk.wait(counter(ws, blockIdx.x, lw == 3 ? C_Q : C_KV), (lw == 3 ? 2 : 4) * sides);
      if (lw == 0) lds_raise(lds + L_GO);
    } else {
      lds_await(lds + L_GO);
    }
    if (lw == 3) dma_lin<10>(qsrc, lane * 16, lbase + L_Q);
    issue();
    __builtin_amdgcn_s_setprio(2);
    WAITV_BARRIER(0);
    lk.done();
    if (late) signal_add(late_cnt);
    if (lw < 3) wait_count(counter(ws, blockIdx.x, C_KV + 1), 4 * sides, lane, 7, 128);
    issue();
    issue();
    const int unit = blockIdx.x * 4 + lw;
    const bool gu = unit < 640;
    int uks, uc;
    {
      const char *s0, *s1;
      unsigned voff;
      int pitch, r0, r1, r2, u3 = -1;
      if (gu) {
        uks = unit >> 5; uc = unit & 31; pitch = FF * 2;
        s0 = wg + min(uc, 30) * 112; s1 = wu + min(uc, 30) * 112;
        voff = (lane >> 3) * pitch + min(lane & 7, uc >= 30 ? 5 : 6) * 16;
        r0 = uks * 32; r1 = 8; r2 = 16;
      } else {
        const int u = unit < 910 ? unit - 640 : unit < 1010 ? unit - 910 : 0;
        uks = u / 5; uc = u - uks * 5; pitch = DM * 2;
        s0 = (unit < 910 ? wd : wo) + uc * 256; s1 = s0 + 128;
        int row = lane >> 3;
        if (unit < 910) { r0 = uks * 56; r1 = 8; r2 = 16; u3 = (uks - 31) * 4; }
        else if (uks < 16) { r0 = uks * 40; r1 = 8; r2 = 20; }
        else { r0 = (uks - 16) * 160 + 16; r1 = 40; r2 = 80; row = (row >> 2) * 20 + (row & 3); }
        voff = row * pitch + (lane & 7) * 16;
      }
#pragma unroll
      for (int i = 0; i < 4; ++i) {
        const int r = u3 < 0 ? r0 + (i & 1) * r1 + (i >> 1) * r2 : (u3 + i) / 3 * 32 + 32 + (u3 + i) * 8;
        dma16(s0 + (size_t)r * pitch, voff, lbase + L_SCR + lw * 8192 + i * 1024);
        dma16(s1 + (size_t)r * pitch, voff, lbase + L_SCR + lw * 8192 + 4096 + i * 1024);
        if (i == 0) WAITV_BARRIER(9);
      }
    }
    WAITV_BARRIER(8);
    issue();
    DRAIN();
    {
      const int p = lane & 3;
      const lchar* const sp = lds + L_SCR + lw * 8192 + (8 * G + ((lane & 15) >> 2)) * 128 + p * 8;
      if (gu) {
        const bf16x8 ones = {0x3f80, 0x3f80, 0x3f80, 0x3f80, 0x3f80, 0x3f80, 0x3f80, 0x3f80};
        const int dst = WS_WGU + (((uc >> 1) * 20 + uks) * 14 + (uc & 1) * 7) * 1024;
        float c0 = 0.f, c1 = 0.f;
#pragma unroll
        for (int t = 0; t < 7; ++t) {
          const lchar* const q = t < 3 ? sp + t * 32 : t < 6 ? sp + 4096 + (t - 3) * 32 : sp + 96 + (p >> 1) * (4096 - 16);
          bf16x8 fr = cat8(tr_read(q), tr_read(q + 512));
          if (uc == 31) fr = (bf16x8){0, 0, 0, 0, 0, 0, 0, 0};
          st128(__builtin_bit_cast(u32x4, fr), rsw, lane * 16, dst + t * 1024);
          const f32x4 cs = MFMA16(ones, fr, ((f32x4){0.f, 0.f, 0.f, 0.f}));
          if (t < 4) c0 = (G == t) ? cs[0] : c0;
          else c1 = (G == t - 4) ? cs[0] : c1;
        }
        const int cp = WS_CSP + (uks * 3584 + uc * 112) * 4;
        __builtin_amdgcn_raw_buffer_store_b32(__float_as_uint(c0), rsw, lane * 4, cp, SC1);
        if (lane < 48) __builtin_amdgcn_raw_buffer_store_b32(__float_as_uint(c1), rsw, lane * 4 + 256, cp, SC1);
      } else if (unit < 1010) {
        const int dst = unit < 910 ? WS_WD : WS_WO, nks = unit < 910 ? 54 : 20;
#pragma unroll
        for (int t = 0; t < 8; ++t) {
          const lchar* const q = sp + (t >> 2) * 4096 + (t & 3) * 32;
          const bf16x8 fr = cat8(tr_read(q), tr_read(q + 512));
          const int t16 = uc * 8 + t, c5 = (t16 * 205) >> 10, tile = t16 - c5 * 5;
          st128(__builtin_bit_cast(u32x4, fr), rsw, lane * 16, dst + ((c5 * nks + uks) * 5 + tile) * 1024);
        }
      }
    }
    WAITV_BARRIER(63);
    issue();
    DRAIN();
    if (lane == 0) signal_add(counter(ws, (blockIdx.x & 7) * 4 + lw, C_W));
    WAITV_BARRIER(63);
    issue();
    if (lw == 3) wait_shared(counter(ws, lane & 31, C_W), 32);
    WAITV_BARRIER(0);
    issue();
    const char* const wop = ws + WS_WO + cb * 100 * 1024;
    auto wo_step = [&](int i) { dma_lin<5>(wop + p3_ks(i, cb) * 5120, lane * 16, lbase + L_WO + i * 5120); };
    float cpart[20];
#pragma unroll
    for (int i = 0; i < 20; ++i)
      cpart[i] = __uint_as_float(__builtin_amdgcn_raw_buffer_load_b32(rsw, min(lane, 55) * 4, WS_CSP + (i * 3584 + cb4 * 224 + lw * 56) * 4, SC1));
    wo_step(4 + lw);
    wo_step(8 + lw);
    wd_regs(0, 0);
    wd_regs(1, 1);
    WAITV_BARRIER(40);
    issue();
    wo_step(12 + lw);
    wo_step(16 + lw);
    wd_regs(2, 2);
    wd_regs(3, 3);
#pragma unroll
    for (int i = 0; i < 20; ++i) csum += cpart[i];
    PIN(csum);
    WAITV_BARRIER(20);
#pragma unroll
    for (int i = 0; i < 3; ++i) {
      const int c = min(i * 64 + lane, 159), row = c / 10;
      dma16(x + (size_t)(rt * 64 + lw * 16) * 1280 + cb * 160, row * 1280 + (c - row * 10) * 16, lbase + L_XR + lw * 3072 + i * 1024);
    }
    wo_step(lw);
    {
      const char* const p = ws + WS_WGU + (blockIdx.x & 3) * 4 * 280 * 1024 + (blockIdx.x >> 3) * 35840 + lw * 8960;
      touch64(p, lane * 128, lbase + 57344);
      touch64(p, min(8192 + lane * 128, 8832), lbase + 57344);
    }
    wd_regs(4, 4);
    wd_regs(5, 5);
    WAITV_BARRIER(17);
    dma_lin<5>(ws + WS_WGU + cb4 * 280 * 1024 + lw * 5120, lane * 16, lbase + L_B4 + lw * 5120);
    WAITV_BARRIER(0);
  }
  const char* const wgu = ws + WS_WGU + cb4 * 280 * 1024;
  dma_lin<9>(wgu + (20 + lw * 9) * 1024, lane * 16, lbase + L_B4 + (20 + lw * 9) * 1024);
  if (lw == 0) {
    Looks lk;
    lk.wait(counter(ws, blockIdx.x, C_O), 32);
    BARRIER();
    DRAIN();
    lk.done();
  } else {
    WAITV_BARRIER(0);
  }
  {
    unsigned* cnt = counter(ws, blk128(rt >> 1, lane & 15), C_H);
    PIN(cnt);
    BARRIER();
    if (lw == 0) { if (lane < 16) signal_add(cnt); }
  }
  u32x2 hres[5];
  {
#pragma unroll
    for (int n = 0; n < 5; ++n)
      hres[n] = __builtin_amdgcn_raw_buffer_load_b64(rsw, unit_off(cb * 20 + n * 4 + G) + (lane & 15) * 16, WS_H + t16 * 20480, SC1);
    if (lane < 56) *LDSP(float, lds + L_CS + (lw * 56 + lane) * 4) = csum;
    dma_lin<7>(wgu + 2 * B4_B + lw * 7168, lane * 16, lbase + L_B4 + 2 * B4_B + lw * 7168);
    const char* ha = ws + WS_H + (rt4 * 8 + lw * 2) * 20480;
    PINS(ha);
    BARRIER();
    int sa_ = 0, sb_ = 0;
    auto issue_a = [&](int s) {
      dma_run<2>(ha + s * 2048, lane * 16, lbase + L_A4 + sa_ + lw * 4096);
      dma_run<2>(ha + 20480 + s * 2048, lane * 16, lbase + L_A4 + sa_ + lw * 4096 + 2048);
      sa_ = (sa_ == 2 * A4_B) ? 0 : sa_ + A4_B;
    };
    issue_a(0);
    issue_a(1);
    u32x2 sp[4];
#pragma unroll
    for (int i = 0; i < 4; ++i)
      sp[i] = __builtin_amdgcn_raw_buffer_load_b64(rsw, ((lane >> 5) * 4 + i) * 16384 + (lane & 31) * 8, WS_ST + (rt4 * 128 + lw * 32) * 8, SC1);
    WAITV_BARRIER(8);
#pragma unroll
    for (int n = 0; n < 5; ++n) PIN(hres[n]);
    {
      constexpr float C0 = -2.0f * 0.7978845608028654f * 1.4426950408889634f, C1 = C0 * 0.044715f;
      const float sum = swap32_add((__uint_as_float(sp[0][0]) + __uint_as_float(sp[1][0])) + (__uint_as_float(sp[2][0]) + __uint_as_float(sp[3][0])));
      const float sq = swap32_add((__uint_as_float(sp[0][1]) + __uint_as_float(sp[1][1])) + (__uint_as_float(sp[2][1]) + __uint_as_float(sp[3][1])));
      const float mu = sum * (1.0f / DM), var = fmaxf(sq * (1.0f / DM) - mu * mu, 0.f) + LN_EPS, r = __builtin_amdgcn_rsqf(var);
      if (lane < 32) *LDSP(f32x4, lds + L_TK + (lw * 32 + lane) * 16) = (f32x4){-mu, C0 * r, C1 * r * r * r, var};
    }
    issue_a(2);
    WAITV_BARRIER(4);
    auto wd_lds = [&](int i) { dma_lin<5>(ws + wd_off(6 + i), lane * 16, lbase + p5_slot(lw, i)); };
#pragma unroll 1
    for (int s = 3; s < 10; ++s) {
      issue_a(s);
      dma_lin<7>(wgu + s * B4_B + lw * 7168, lane * 16, lbase + L_B4 + sb_ + lw * 7168);
      sb_ = (sb_ == 2 * B4_B) ? 0 : sb_ + B4_B;
      dma_run<1>(ws + wd_off(6) + min(s - 3, 4) * 1024, lane * 16, lbase + p5_slot(lw, 0) + min(s - 3, 4) * 1024);
      WAITV_BARRIER(12);
    }
    wd_lds(1);
    {
      const int line = ((blockIdx.x >> 3) & 3) * 30 + min(lane, 29), st = line / 40;
      int o0 = wd_off(11), o1 = wd_off(12), o2 = wd_off(13);
      PINS(o0); PINS(o1); PINS(o2);
      touch64(ws, (st == 0 ? o0 : st == 1 ? o1 : o2) + (line - st * 40) * 128, lbase + L_TK + 2048);
    }
    if (lw < 3) { wd_lds(3); WAITV_BARRIER(12); } else { WAITV_BARRIER(7); wd_lds(3); }
    wd_lds(2);
    wd_lds(4);
  }
  {
    const int tok = t16 * 16 + (lane & 15);
    f32x4 acc[4][5] = {};
#pragma unroll
    for (int n = 0; n < 5; ++n) {
      const u32x2 r = hres[n];
      acc[0][n] = (f32x4){bf_lo(r[0]), bf_hi(r[0]), bf_lo(r[1]), bf_hi(r[1])};
    }
#pragma unroll
    for (int m = 0; m < 4; ++m)
#pragma unroll
      for (int n = 0; n < 5; ++n) PIN(acc[m][n]);
    const lchar* ap[2][4];
#pragma unroll
    for (int m = 0; m < 4; ++m) {
      ap[0][m] = lds + (((lw + m) & 3) * 4 + lw) * 1024 + lane * 16;
      ap[1][m] = ap[0][m] + L_A4;
      PIN(ap[0][m]); PIN(ap[1][m]);
    }
    {
      const char* src[3];
#pragma unroll
      for (int i = 0; i < 3; ++i) { src[i] = ws + WS_A + t16 * 54 * 1024 + p5_grp(i, cb) * 4096; PINS(src[i]); }
      lchar* const go = lds + L_TK + 2048 + 256;
      *LDSP(volatile unsigned, go) = 0;
      BARRIER();
      Looks lk;
      if (lw == 0) {
        lk.wait(counter(ws, blockIdx.x, C_A), 32);
        lds_raise(go);
      } else {
        DRAIN();
        lds_await(go);
      }
      dma_run<4>(src[0], lane * 16, lbase + lw * 4096 + p5_tok(0));
      dma_run<4>(src[1], lane * 16, lbase + lw * 4096 + p5_tok(1));
      asm volatile("s_waitcnt vmcnt(4)" ::: "memory");
      lk.done();
      dma_run<4>(src[2], lane * 16, lbase + lw * 4096 + p5_tok(2));
      BARRIER();
    }
    bf16x8 af[2][4];
#define P5_RA(i, m) af[(i) & 1][m] = *LDSP(const bf16x8, ap[(i) % 3 == 1][m] + ((i) % 3 == 2 ? 16384 : 0))
#pragma unroll
    for (int m = 0; m < 4; ++m) { P5_RA(0, m); SBAR(); }
#pragma unroll
    for (int i = 0; i < 13; ++i) {
      const lchar* wl = lds + lane * 16;
      if (i >= 1 && i < 6) wl += p5_slot(lw, i - 1);
      if (i >= 9 && i < 12) wl += p5_slot(lw, i - 9);
      PIN(wl);
#pragma unroll
      for (int n = 0; n < 5; ++n)
#pragma unroll
        for (int m = 0; m < 4; ++m) {
          SBAR();
          MFMA16_ACC(acc[m][n], wr[i % 6][n], af[i & 1][m]);
          SBAR();
          if (n == 0 && m == 3) {
            if (i == 0) asm volatile("s_waitcnt vmcnt(4)" ::: "memory");
            if (i == 1) DRAIN();
            asm volatile("s_barrier" ::: "memory");
          }
          if (n == 1) P5_RA(i + 1, m);
          const int j = (n - 2) * 2 + (m >> 1);
          if (n >= 2 && (m & 1) && j < 5) {
            if (i >= 1 && i < 6) wr[i - 1][j] = *LDSP(const u32x4, wl + j * 1024);
            if (i >= 9 && i < 12) wr[(i + 2) % 6][j] = *LDSP(const u32x4, wl + j * 1024);
          }
        }
    }
    lchar* xw[3];
    xw[0] = lds + p5_slot(lw, 3) + lane * 16; xw[1] = lds + p5_slot(lw, 4) + lane * 16; xw[2] = lds + lw * 5120 + lane * 16;
    if (lw < 2) {
#pragma unroll
      for (int m = 1; m < 5; ++m)
#pragma unroll
        for (int n = 0; n < 5; ++n) {
          SBAR();
          MFMA16_ACC(acc[m & 3][n], wr[1][n], af[1][m & 3]);
          SBAR();
          if (m >= 3) *LDSP(f32x4, xw[m - 3] + n * 1024) = acc[m - 2][n];
        }
      MFMA_DONE();
      SBAR();
    } else {
      MFMA_DONE();
      SBAR();
#pragma unroll
      for (int m = 1; m < 3; ++m)
#pragma unroll
        for (int n = 0; n < 5; ++n) *LDSP(f32x4, xw[m - 1] + n * 1024) = acc[m][n];
    }
#undef P5_RA
#pragma unroll
    for (int n = 0; n < 5; ++n) *LDSP(f32x4, xw[2] + n * 1024) = acc[3][n];
    const lchar* xr[3];
    xr[0] = lds + p5_slot((lw - 1) & 3, 3) + lane * 16; xr[1] = lds + p5_slot((lw - 2) & 3, 4) + lane * 16; xr[2] = lds + ((lw - 3) & 3) * 5120 + lane * 16;
    PIN(xr[0]); PIN(xr[1]); PIN(xr[2]);
    BARRIER();
    u32x2 o[5];
#pragma unroll
    for (int n = 0; n < 5; ++n) {
      const f32x4 v = (acc[0][n] + *LDSP(const f32x4, xr[0] + n * 1024)) + (*LDSP(const f32x4, xr[1] + n * 1024) + *LDSP(const f32x4, xr[2] + n * 1024));
      o[n] = (u32x2){pk_bf16(v[0], v[1]), pk_bf16(v[2], v[3])};
    }
    char* const op = out + (size_t)tok * 1280 + cb * 160;
#pragma unroll
    for (int np = 0; np < 2; ++np) *(u32x4*)(op + ((2 * np + (G & 1)) * 4 + (G & 2)) * 8) = widen16(o[2 * np], o[2 * np + 1]);
    *(u32x2*)(op + (16 + G) * 8) = o[4];
  }
}

extern "C" __global__ __launch_bounds__(512, 2) void block_fwd(const char* __restrict__ x, const char* __restrict__ wqkv, const char* __restrict__ wo,
                                                               const char* __restrict__ wg, const char* __restrict__ wu,
                                                               const char* __restrict__ wd, char* ws, char* __restrict__ out) {
  __shared__ __attribute__((aligned(1024))) char smem[163840];
  const int lane = threadIdx.x & 63, wave = __builtin_amdgcn_readfirstlane(threadIdx.x >> 6);
  if (blockIdx.x < 8) {
    u64 c0, c1;
    asm volatile("s_getpc_b64 s[20:21]\n\ts_add_u32 s20, s20, block_fwd@rel32@lo+4\n\ts_addc_u32 s21, s21, block_fwd@rel32@hi+12" : "={s[20:21]}"(c0)::"scc");
    asm volatile("s_getpc_b64 s[22:23]\n\ts_add_u32 s22, s22, block_end@rel32@lo+4\n\ts_addc_u32 s23, s23, block_end@rel32@hi+12" : "={s[22:23]}"(c1)::"scc");
    const unsigned len = (unsigned)(c1 - c0);
    if (len < 65536) touch64((const void*)c0, min(threadIdx.x * 128, len), (unsigned)(uintptr_t)(lchar*)smem + L_RS + 1536);
  }
  const int bj = (blockIdx.x >> 3) - 2;
  const int ti = bj < 10 ? (blockIdx.x & 1) * 10 + bj : (blockIdx.x & 1) * 20 + 10 + bj, rt = ((blockIdx.x & 7) >> 1) * 4 + (ti & 3), ct = blockIdx.x < 16 ? 15 : ti >> 2;
  QkvLoader ld(x, wqkv, rt, ct, wave & 3, lane);
  u32x2 v[5] = {};
  if (wave < 4) {
#pragma unroll
    for (int i = 0; i < 5; ++i) v[i] = *(const u32x2*)(x + (size_t)stat_row(wave, lane) * 1280 + (i * 32 + (lane & 31)) * 8);
  } else if (ct < 15) {
    ld.issue((unsigned)(uintptr_t)(lchar*)smem);
  }
  asm volatile("" ::: "memory");
  if (wave >= 4) role_b((lchar*)smem, wave - 4, lane, ld, rt, ct, x, wo, wg, wu, wd, ws, out);
  else role_a((lchar*)smem, wave, lane, v, rt, ct, ws);
}
extern "C" __global__ void block_end() {}

extern "C" void kernel_launch(void* const* d_in, const int* in_sizes, int n_in, void* d_out, int out_size, void* d_ws, size_t ws_size,
                              hipStream_t stream) {
  block_fwd<<<256, 512, 0, stream>>>((const char*)d_in[0], (const char*)d_in[1], (const char*)d_in[2], (const char*)d_in[4],
                                     (const char*)d_in[5], (const char*)d_in[6], (char*)d_ws, (char*)d_out);
}
```
